```python
import torch

def _softmax(x, axis=-1):
    return torch.softmax(x, dim=axis)
def _gelu(x):
    return torch.nn.functional.gelu(x, approximate="tanh")
def _silu(x):
    return torch.nn.functional.silu(x)
def _sigmoid(x):
    return torch.sigmoid(x)
def _layernorm(x, axis=-1, eps=1e-5):
    m = x.mean(dim=axis, keepdim=True)
    v = x.var(dim=axis, keepdim=True, unbiased=False)
    return (x - m) / torch.sqrt(v + eps)
def _scatter_add(tgt, idx, src, axis=0):
    return tgt.clone().index_add_(axis, idx, src)
def _segment_sum(src, seg, n):
    out = torch.zeros((n,) + src.shape[1:], dtype=src.dtype)
    return out.index_add_(0, seg, src)
def _pad(x, pw):
    flat = tuple(v for pair in reversed(pw) for v in pair)
    return torch.nn.functional.pad(x, flat)

kernel_name = 'gated_mix__mha_block__mixer_s_mlp__prompt'

def setup_inputs():

    _w = (lambda a: (lambda t: t.to(torch.bfloat16) if t.is_floating_point() else t)(torch.as_tensor(a)))
    def _A_setup():
        B, S, D, H = 1, 2048, 1024, 32
        return {
            "x": _w((lambda *s: torch.randn(*s, dtype=torch.bfloat16) * 0.1)(B, S, D)),
            "wqkv": _w((lambda *s: torch.randn(*s, dtype=torch.bfloat16) * 0.4)(D, 3 * D)),
            "wo": _w((lambda *s: torch.randn(*s, dtype=torch.bfloat16) * 0.4)(D, D)),
        }
    _A_in = _A_setup()

    def _B_setup():
        B, S, D, F = 1, 2048, 1024, 3584
        return {
            "x":  _w((lambda *s: torch.randn(*s, dtype=torch.bfloat16) * 0.1)(B, S, D)),
            "w1": _w((lambda *s: torch.randn(*s, dtype=torch.bfloat16) * 3.2)(D, D) * (D ** -0.5)),
            "w2": _w((lambda *s: torch.randn(*s, dtype=torch.bfloat16) * 3.2)(D, D) * (D ** -0.5)),
            "w3": _w((lambda *s: torch.randn(*s, dtype=torch.bfloat16) * 3.2)(D, F) * (D ** -0.5)),
            "w4": _w((lambda *s: torch.randn(*s, dtype=torch.bfloat16) * 3.2)(F, D) * (F ** -0.5)),
        }
    _B_in = _B_setup()

    return {
        "A_x": _A_in["x"],
        "A_wqkv": _A_in["wqkv"],
        "A_wo": _A_in["wo"],
        "B_x": _B_in["x"],
        "B_w1": _B_in["w1"],
        "B_w2": _B_in["w2"],
        "B_w3": _B_in["w3"],
        "B_w4": _B_in["w4"],
        "gate_w": _w((lambda *s: torch.randn(*s, dtype=torch.bfloat16) * 0.4)(1024) * 16.0),
    }

def reference(A_x, A_wqkv, A_wo, B_x, B_w1, B_w2, B_w3, B_w4, gate_w):
    def _A_ref(x, wqkv, wo):
        B, S, D = x.shape
        H = 32
        Dh = D // H
        qkv = (lambda x, *d: x.permute(*d))((x @ wqkv).reshape(B, S, 3, H, Dh), 2, 0, 3, 1, 4)
        q, k, v = qkv[0], qkv[1], qkv[2]
        scores = torch.einsum("bhqd,bhkd->bhqk", q, k) * (Dh ** -0.5)
        p = _softmax(scores, axis=-1)
        o = torch.einsum("bhqk,bhkd->bhqd", p, v)
        return (lambda x, *d: x.permute(*d))(o, 0, 2, 1, 3).reshape(B, S, D) @ wo

    def _B_ref(x, w1, w2, w3, w4):
        B, S, D = x.shape
        h = _gelu(_layernorm(x, axis=-1) @ w1)
        c = D // 4
        g0 = _pad(h[:, :, 0*c:1*c], ((0, 0), (1, 0), (0, 0)))[:, :S, :]
        g1 = _pad(h[:, :, 1*c:2*c], ((0, 0), (0, 1), (0, 0)))[:, 1:, :]
        g2 = _pad(h[:, :, 2*c:3*c], ((0, 0), (2, 0), (0, 0)))[:, :S, :]
        g3 = _pad(h[:, :, 3*c:   ], ((0, 0), (0, 2), (0, 0)))[:, 2:, :]
        hs = torch.concatenate([g0, g1, g2, g3], axis=-1) @ w2
        x1 = x + hs
        h2 = _gelu(_layernorm(x1, axis=-1) @ w3) @ w4
        return x1 + h2

    x = A_x
    g = _sigmoid((x * gate_w).sum(-1, keepdims=True))
    a = _A_ref(x, A_wqkv, A_wo)
    b = _B_ref(x, B_w1, B_w2, B_w3, B_w4)
    return g * a + (1 - g) * b
```

```cpp
#include <hip/hip_runtime.h>
#include <stdint.h>

typedef __attribute__((ext_vector_type(8))) __bf16 bf16x8;
typedef __attribute__((ext_vector_type(4))) __bf16 bf16x4;
typedef __attribute__((ext_vector_type(2))) __bf16 bf16x2;
typedef __attribute__((ext_vector_type(4))) short s16x4;
typedef __attribute__((ext_vector_type(16))) float f32x16;
typedef __attribute__((ext_vector_type(4))) float f32x4;
typedef __attribute__((ext_vector_type(2))) float f32x2;
typedef __attribute__((ext_vector_type(4))) unsigned u32x4;
typedef __attribute__((ext_vector_type(2))) unsigned u32x2;
#define LAS __attribute__((address_space(3)))
#define DEV __device__ __forceinline__

#define WAIT_V(n) asm volatile("s_waitcnt vmcnt(" #n ")" ::: "memory")
#define WAIT_L(n) asm volatile("s_waitcnt lgkmcnt(" #n ")" ::: "memory")
#define BAR() __builtin_amdgcn_s_barrier()
#define SCHED() __builtin_amdgcn_sched_barrier(0)

constexpr int S_ = 2048, D_ = 1024, F_ = 3584, KCAT = D_ + F_;
constexpr size_t WS_Q = 0;
constexpr size_t WS_K = WS_Q + (size_t)4 * 1024 * 1024;
constexpr size_t WS_V = WS_K + (size_t)4 * 1024 * 1024;
constexpr size_t WS_HS = WS_V + (size_t)4 * 1024 * 1024;
constexpr size_t WS_X1 = WS_HS + (size_t)4 * 1024 * 1024;
constexpr size_t WS_AC = WS_X1 + (size_t)4 * 1024 * 1024;
constexpr size_t WS_ST1 = WS_AC + (size_t)S_ * KCAT * 2;
constexpr size_t WS_ST2 = WS_ST1 + (size_t)S_ * 16;

DEV int perm23(int i) { return (i & ~12) | ((i & 4) << 1) | ((i & 8) >> 1); }
DEV unsigned pk2(float a, float b) {
  bf16x2 v; v[0] = (__bf16)a; v[1] = (__bf16)b;
  return __builtin_bit_cast(unsigned, v);
}
DEV float bflo(unsigned u) { return __builtin_bit_cast(float, u << 16); }
DEV float bfhi(unsigned u) { return __builtin_bit_cast(float, u & 0xffff0000u); }
DEV float rbf(float a) { return (float)(__bf16)a; }
#define GLDS_ "global_load_lds_dwordx4 "
DEV void glds1(const char* sb, unsigned l, unsigned v0) {
  asm volatile("s_mov_b32 m0, %1\n\ts_nop 0\n\t" GLDS_ "%2, %0" ::"s"(sb), "s"(l), "v"(v0) : "memory");
}
DEV void glds2(const char* sb, unsigned l, unsigned v0, unsigned v1) {
  asm volatile("s_mov_b32 m0, %1\n\ts_nop 0\n\t" GLDS_ "%2, %0\n\t" GLDS_ "%3, %0 offset:1024" ::"s"(sb), "s"(l), "v"(v0), "v"(v1) : "memory");
}
DEV void glds4(const char* sb, unsigned l, unsigned v0, unsigned v1, unsigned v2, unsigned v3) {
  asm volatile("s_mov_b32 m0, %1\n\ts_nop 0\n\t" GLDS_ "%2, %0\n\t" GLDS_ "%3, %0 offset:1024\n\t" GLDS_ "%4, %0 offset:2048\n\t" GLDS_ "%5, %0 offset:3072"
               ::"s"(sb), "s"(l), "v"(v0), "v"(v1), "v"(v2), "v"(v3) : "memory");
}
template <int N>
DEV void gldsN(const char* sb, unsigned l, const unsigned (&v)[N]) {
  if constexpr (N == 1) glds1(sb, l, v[0]);
  else if constexpr (N == 2) glds2(sb, l, v[0], v[1]);
  else if constexpr (N == 4) glds4(sb, l, v[0], v[1], v[2], v[3]);
  else static_assert(N < 0, "glds group");
}
DEV float gelu_tanh(float u) {
  const float c1 = -2.0f * 0.7978845608028654f * 1.4426950408889634f;
  const float c2 = c1 * 0.044715f;
  float t = __builtin_fmaf(u * u, c2, c1);
  float e = __builtin_amdgcn_exp2f(u * t);
  return u * __builtin_amdgcn_rcpf(1.0f + e);
}
template <int N> DEV void wait_vm() {
  if constexpr (N == 0) WAIT_V(0);
  else if constexpr (N == 3) WAIT_V(3);
  else if constexpr (N == 4) WAIT_V(4);
  else if constexpr (N == 6) WAIT_V(6);
  else if constexpr (N == 8) WAIT_V(8);
  else if constexpr (N == 9) WAIT_V(9);
  else if constexpr (N == 12) WAIT_V(12);
  else if constexpr (N == 16) WAIT_V(16);
  else if constexpr (N == 18) WAIT_V(18);
  else if constexpr (N == 24) WAIT_V(24);
  else if constexpr (N == 30) WAIT_V(30);
  else static_assert(N < 0, "add wait");
}

template <int BM, int BN, int WM, int WN, int NS, int AMODE, bool SWAP, bool COLSUM>
struct Gemm {
  static constexpr int NW = WM * WN, TM = BM / WM, TN = BN / WN, MT = TM / 32, NT = TN / 32;
  static constexpr int ABYTES = BM * 128, BBYTES = BN * 128, STAGE = ABYTES + BBYTES;
  static constexpr int LA = BM / 8 / NW, LB = BN / 8 / NW, LT = LA + LB;
  static constexpr int RB = BN * 2;
  static DEV int bswz(int row) { return BN == 64 ? (((row >> 1) & 1) << 2) : (BN % 128 == 0 ? ((row & 3) << 2) : 0); }

  static DEV void run(LAS char* lds, const char* ap, unsigned astr, const char* bp0, const char* bp1, int ksw,
                      unsigned bstr, int nk, f32x16 (&acc)[MT][NT], float (&cs)[NT]) {
    const int tid = threadIdx.x, lane = tid & 63;
    const int wid = __builtin_amdgcn_readfirstlane(tid >> 6);
    const int wm = wid / WN, wn = wid % WN;
    const int h = lane >> 5, pr = perm23(lane & 31);
    unsigned voa[LA], vob[LB];
#pragma unroll
    for (int i = 0; i < LA; ++i) {
      const int j = wid * LA + i;
      if (AMODE == 0) {
        const int row = 8 * j + (lane >> 3);
        voa[i] = row * astr + (((lane & 7) ^ ((row >> 1) & 7)) << 4) - i * 1024;
      } else {
        const int j0 = wid * LA;
        voa[i] = (j0 >> 2) * astr + (j0 & 3) * 1024 + lane * 16;
      }
    }
#pragma unroll
    for (int i = 0; i < LB; ++i) {
      const int id = (wid * LB + i) * 64 + lane, row = id / (BN / 8), pos = id % (BN / 8);
      vob[i] = row * bstr + ((pos ^ bswz(row)) << 4) - i * 1024;
    }
    int ra;
    if (AMODE == 0) {
      const int row = wm * TM + pr;
      ra = row * 128 + ((h ^ ((row >> 1) & 7)) << 4);
    } else {
      ra = wm * MT * 4096 + lane * 16;
    }
    int rb[NT];
    {
      const int blk = (lane >> 4) & 1, q = (lane >> 2) & 3, p = lane & 3, pp = ((p & 1) << 1) | (p >> 1);
#pragma unroll
      for (int ni = 0; ni < NT; ++ni) {
        const int ch = (wn * TN + ni * 32) / 8 + blk * 2 + (pp >> 1);
        rb[ni] = ABYTES + (8 * h + q) * RB + ((ch ^ bswz(q)) << 4) + (pp & 1) * 8;
      }
    }
    auto issue = [&](int kt, int buf) {
      const char* a = ap + (size_t)kt * (AMODE == 0 ? 128 : 4096);
      const char* b = (kt < ksw ? bp0 : bp1) + (size_t)kt * 64 * bstr;
      const unsigned l = (unsigned)(uintptr_t)lds + buf * STAGE;
      gldsN<LA>(a, l + wid * LA * 1024, voa);
      gldsN<LB>(b, l + ABYTES + wid * LB * 1024, vob);
    };
    bf16x8 xf[2][MT], wf[2][NT];
    auto rdfrag = [&](int fb, int buf, int s) {
      LAS char* l = lds + buf * STAGE;
#pragma unroll
      for (int ni = 0; ni < NT; ++ni) {
        s16x4 lo = __builtin_amdgcn_ds_read_tr16_b64_v4i16((LAS s16x4*)(l + rb[ni] + s * 16 * RB));
        s16x4 hi = __builtin_amdgcn_ds_read_tr16_b64_v4i16((LAS s16x4*)(l + rb[ni] + s * 16 * RB + 4 * RB));
        u32x2 a = __builtin_bit_cast(u32x2, lo), b = __builtin_bit_cast(u32x2, hi);
        u32x4 c; c[0] = a[0]; c[1] = a[1]; c[2] = b[0]; c[3] = b[1];
        wf[fb][ni] = __builtin_bit_cast(bf16x8, c);
      }
#pragma unroll
      for (int mi = 0; mi < MT; ++mi) {
        if (AMODE == 0) xf[fb][mi] = *(LAS bf16x8*)(l + (ra ^ (s << 5)) + mi * 4096);
        else xf[fb][mi] = *(LAS bf16x8*)(l + ra + mi * 4096 + s * 1024);
      }
    };
    auto mma = [&](int fb, int s) {
#pragma unroll
      for (int mi = 0; mi < MT; ++mi)
#pragma unroll
        for (int ni = 0; ni < NT; ++ni) {
          if (SWAP) acc[mi][ni] = __builtin_amdgcn_mfma_f32_32x32x16_bf16(wf[fb][ni], xf[fb][mi], acc[mi][ni], 0, 0, 0);
          else acc[mi][ni] = __builtin_amdgcn_mfma_f32_32x32x16_bf16(xf[fb][mi], wf[fb][ni], acc[mi][ni], 0, 0, 0);
        }
      if (COLSUM) {
        if ((wm & 3) == s || WM < 4) {
          bf16x2 one; one[0] = (__bf16)1.0f; one[1] = (__bf16)1.0f;
#pragma unroll
          for (int ni = 0; ni < NT; ++ni) {
            u32x4 w = __builtin_bit_cast(u32x4, wf[fb][ni]);
#pragma unroll
            for (int e = 0; e < 4; ++e) {
              const unsigned we = w[e];
              cs[ni] = __builtin_amdgcn_fdot2_f32_bf16(__builtin_bit_cast(bf16x2, we), one, cs[ni], false);
            }
          }
        }
      }
    };
#pragma unroll
    for (int i = 0; i < NS; ++i) issue(i, i);
    wait_vm<(NS - 1) * LT>();
    BAR();
    rdfrag(0, 0, 0);
    int buf = 0;
#pragma unroll 1
    for (int kt = 0; kt < nk; ++kt) {
      rdfrag(1, buf, 1); SCHED(); mma(0, 0); SCHED();
      rdfrag(0, buf, 2); SCHED(); mma(1, 1); SCHED();
      rdfrag(1, buf, 3); SCHED(); mma(0, 2); SCHED();
      WAIT_L(0);
      if (kt + NS - 1 < nk) wait_vm<(NS - 2) * LT>(); else wait_vm<0>();
      BAR();
      if (kt + NS < nk) issue(kt + NS, buf);
      buf = (buf == NS - 1) ? 0 : buf + 1;
      SCHED();
      rdfrag(0, buf, 0);
      SCHED(); mma(1, 3); SCHED();
    }
    WAIT_L(0);
  }
};

DEV u32x4 pack8(const f32x16& a, int s, float sc) {
  u32x4 r;
#pragma unroll
  for (int e = 0; e < 4; ++e) r[e] = pk2(a[8 * s + 2 * e] * sc, a[8 * s + 2 * e + 1] * sc);
  return r;
}

DEV float wave_sum(float v) {
#pragma unroll
  for (int o = 32; o >= 1; o >>= 1) v += __shfl_xor(v, o);
  return v;
}
__global__ __launch_bounds__(256) void k_prep(const char* __restrict__ x, const char* __restrict__ gw, char* __restrict__ ws) {
  const int lane = threadIdx.x & 63, row = blockIdx.x * 4 + (threadIdx.x >> 6);
  u32x4 a = *(const u32x4*)(x + (size_t)row * 2048 + lane * 16), b = *(const u32x4*)(x + (size_t)row * 2048 + 1024 + lane * 16);
  u32x4 ga = *(const u32x4*)(gw + lane * 16), gb = *(const u32x4*)(gw + 1024 + lane * 16);
  float v[16], w[16];
#pragma unroll
  for (int e = 0; e < 4; ++e) {
    v[2 * e] = bflo(a[e]); v[2 * e + 1] = bfhi(a[e]); v[8 + 2 * e] = bflo(b[e]); v[9 + 2 * e] = bfhi(b[e]);
    w[2 * e] = bflo(ga[e]); w[2 * e + 1] = bfhi(ga[e]); w[8 + 2 * e] = bflo(gb[e]); w[9 + 2 * e] = bfhi(gb[e]);
  }
  float s = 0.f, d = 0.f;
#pragma unroll
  for (int e = 0; e < 16; ++e) { s += v[e]; d += rbf(v[e] * w[e]); }
  s = wave_sum(s); d = wave_sum(d);
  const float mu = s * (1.0f / 1024.0f);
  float q = 0.f;
#pragma unroll
  for (int e = 0; e < 16; ++e) { const float t = v[e] - mu; q += t * t; }
  q = wave_sum(q);
  if (lane == 0) {
    const float rstd = rsqrtf(q * (1.0f / 1024.0f) + 1e-5f);
    const float z = rbf(d);
    const float g = rbf(1.0f / (1.0f + __expf(-z)));
    f32x4 o; o[0] = mu; o[1] = rstd; o[2] = g; o[3] = rbf(1.0f - g);
    *(f32x4*)(ws + WS_ST1 + (size_t)row * 16) = o;
  }
}

template <int BN, int WM, int WN, int NT>
DEV void colsum_reduce(LAS char* lds, const float (&cs)[NT], int wm, int wn, int lane) {
  LAS float* part = (LAS float*)(lds + 4096);
#pragma unroll
  for (int ni = 0; ni < NT; ++ni) part[(wm * 2 + (lane >> 5)) * BN + wn * NT * 32 + ni * 32 + perm23(lane & 31)] = cs[ni];
  WAIT_L(0); BAR();
  if ((int)threadIdx.x < BN) {
    float t = 0.f;
#pragma unroll
    for (int i = 0; i < WM * 2; ++i) t += part[i * BN + threadIdx.x];
    ((LAS float*)lds)[threadIdx.x] = t;
  }
  WAIT_L(0); BAR();
}

__global__ __launch_bounds__(512) void k_qkv_w1(const char* __restrict__ x, const char* __restrict__ wqkv,
                                                const char* __restrict__ w1, char* __restrict__ ws) {
  extern __shared__ __attribute__((aligned(16))) char smem[];
  LAS char* lds = (LAS char*)smem;
  const int bid = blockIdx.x, xcd = bid & 7, idx = bid >> 3;
  const int pm = (xcd & 1) * 4 + (idx & 3), sec = xcd >> 1, pn = idx >> 2;
  const int lane = threadIdx.x & 63, wid = __builtin_amdgcn_readfirstlane(threadIdx.x >> 6), wm = wid >> 1, wn = wid & 1;
  f32x16 acc[2][2] = {};
  float cs[2] = {0.f, 0.f};
  const char* ap = x + (size_t)pm * 256 * 2048;
  if (sec == 3) {
    const char* bp = w1 + pn * 256;
    Gemm<256, 128, 4, 2, 3, 0, true, true>::run(lds, ap, 2048, bp, bp, 1 << 20, 2048, 16, acc, cs);
    BAR();
    colsum_reduce<128, 4, 2, 2>(lds, cs, wm, wn, lane);
    const int grp = pn >> 1, dsh = (grp & 1) ? -(1 + (grp >> 1)) : (1 + (grp >> 1));
#pragma unroll
    for (int mi = 0; mi < 2; ++mi) {
      const int m = pm * 256 + wm * 64 + mi * 32 + perm23(lane & 31);
      const f32x4 st = *(const f32x4*)(ws + WS_ST1 + (size_t)m * 16);
      const int md = m + dsh;
      const bool ok = (unsigned)md < 2048u;
      const int mw = md & 2047;
      char* dst = ws + WS_HS + (size_t)(mw >> 5) * 65536 + (perm23(mw & 31) + (lane & 32)) * 16;
      const float nmu = -st[0], rs = st[1];
#pragma unroll
      for (int ni = 0; ni < 2; ++ni)
#pragma unroll
        for (int s = 0; s < 2; ++s) {
          const int nl = wn * 64 + ni * 32 + s * 16 + (lane >> 5) * 8;
          const f32x4 c0 = *(LAS f32x4*)(lds + nl * 4), c1 = *(LAS f32x4*)(lds + nl * 4 + 16);
          float hv[8];
#pragma unroll
          for (int e = 0; e < 8; ++e) {
            const float c = e < 4 ? c0[e & 3] : c1[e & 3];
            hv[e] = gelu_tanh(rs * __builtin_fmaf(nmu, c, acc[mi][ni][8 * s + e]));
          }
          u32x4 o;
#pragma unroll
          for (int e = 0; e < 4; ++e) o[e] = ok ? pk2(hv[2 * e], hv[2 * e + 1]) : 0u;
          *(u32x4*)(dst + (size_t)(pn * 8 + wn * 4 + ni * 2 + s) * 1024) = o;
        }
    }
  } else {
    const char* bp = wqkv + sec * 2048 + pn * 256;
    if (sec == 2) Gemm<256, 128, 4, 2, 3, 0, false, false>::run(lds, ap, 2048, bp, bp, 1 << 20, 6144, 16, acc, cs);
    else Gemm<256, 128, 4, 2, 3, 0, true, false>::run(lds, ap, 2048, bp, bp, 1 << 20, 6144, 16, acc, cs);
    const float sc = sec == 0 ? 0.17677669529663687f * 1.4426950408889634f : 1.0f;
    char* dst = ws + WS_Q + (size_t)sec * 4194304 + lane * 16;
#pragma unroll
    for (int mi = 0; mi < 2; ++mi)
#pragma unroll
      for (int ni = 0; ni < 2; ++ni) {
        const int head = pn * 4 + wn * 2 + ni, mb = pm * 8 + wm * 2 + mi;
#pragma unroll
        for (int s = 0; s < 2; ++s) *(u32x4*)(dst + (size_t)((head * 64 + mb) * 2 + s) * 1024) = pack8(acc[mi][ni], s, sc);
      }
  }
}

__global__ __launch_bounds__(512) void k_attn(char* __restrict__ ws) {
  extern __shared__ __attribute__((aligned(16))) char smem[];
  LAS char* lds = (LAS char*)smem;
  const int bid = blockIdx.x, head = (bid & 7) * 4 + ((bid >> 3) & 3), qg = bid >> 5;
  const int lane = threadIdx.x & 63, wid = __builtin_amdgcn_readfirstlane(threadIdx.x >> 6);
  const int half = wid >> 2, qw = wid & 3;
  const char* Qh = ws + WS_Q + (size_t)head * 131072;
  const char* Kh = ws + WS_K + (size_t)head * 131072;
  const char* src = Kh + (size_t)(qw >> 1) * 4194304 + half * 65536 + (qw & 1) * 4096;
  const unsigned dstw = (unsigned)(uintptr_t)lds + half * 16384 + qw * 4096, l16 = lane * 16;
  auto issue = [&](int st) { glds4(src + st * 8192, dstw + (st & 3) * 32768, l16, l16, l16, l16); };
  bf16x8 qf[2][2];
  const int mb0 = qg * 8 + qw * 2;
#pragma unroll
  for (int qb = 0; qb < 2; ++qb)
#pragma unroll
    for (int s = 0; s < 2; ++s) qf[qb][s] = *(const bf16x8*)(Qh + (size_t)((mb0 + qb) * 2 + s) * 1024 + lane * 16);
  bf16x8 sel;
  {
    const unsigned one2 = ((lane & 15) == ((lane >> 4) & 1)) ? 0x3f803f80u : 0u;
    u32x4 t; t[0] = one2; t[1] = one2; t[2] = one2; t[3] = one2;
    sel = __builtin_bit_cast(bf16x8, t);
  }
  float mrow[2];
  f32x16 o[2];
  float lrow[2];
  const int rbase = half * 16384 + lane * 16;
#pragma unroll 1
  for (int attempt = 0; attempt < 2; ++attempt) {
    issue(0); issue(1); issue(2); issue(3);
    if (attempt == 0) {
      const char* kp = Kh + half * 65536 + lane * 16;
      const bf16x8 k0 = *(const bf16x8*)kp, k1 = *(const bf16x8*)(kp + 1024);
#pragma unroll
      for (int qb = 0; qb < 2; ++qb) {
        f32x16 s = {};
        s = __builtin_amdgcn_mfma_f32_32x32x16_bf16(k0, qf[qb][0], s, 0, 0, 0);
        s = __builtin_amdgcn_mfma_f32_32x32x16_bf16(k1, qf[qb][1], s, 0, 0, 0);
        float m = s[0];
#pragma unroll
        for (int e = 1; e < 16; ++e) m = fmaxf(m, s[e]);
        mrow[qb] = fmaxf(m, __shfl_xor(m, 32));
      }
    } else {
      float m[2] = {-INFINITY, -INFINITY};
#pragma unroll 1
      for (int t = 0; t < 32; ++t) {
        const char* kp = Kh + half * 65536 + t * 2048 + lane * 16;
        const bf16x8 k0 = *(const bf16x8*)kp, k1 = *(const bf16x8*)(kp + 1024);
#pragma unroll
        for (int qb = 0; qb < 2; ++qb) {
          f32x16 s = {};
          s = __builtin_amdgcn_mfma_f32_32x32x16_bf16(k0, qf[qb][0], s, 0, 0, 0);
          s = __builtin_amdgcn_mfma_f32_32x32x16_bf16(k1, qf[qb][1], s, 0, 0, 0);
#pragma unroll
          for (int e = 0; e < 16; ++e) m[qb] = fmaxf(m[qb], s[e]);
        }
      }
#pragma unroll
      for (int qb = 0; qb < 2; ++qb) mrow[qb] = fmaxf(m[qb], __shfl_xor(m[qb], 32));
    }
    f32x16 negm[2];
    f32x4 l[2];
#pragma unroll
    for (int qb = 0; qb < 2; ++qb) {
#pragma unroll
      for (int e = 0; e < 16; ++e) { negm[qb][e] = -mrow[qb]; o[qb][e] = 0.f; }
      l[qb] = (f32x4){0.f, 0.f, 0.f, 0.f};
    }
    WAIT_V(0);
    BAR();
    bf16x8 kf[2][2], vf[2][2];
    auto rd = [&](int fb, int slot, int t) {
      LAS char* p = lds + rbase + slot * 32768 + t * 2048;
      kf[fb][0] = *(LAS bf16x8*)(p); kf[fb][1] = *(LAS bf16x8*)(p + 1024);
      vf[fb][0] = *(LAS bf16x8*)(p + 8192); vf[fb][1] = *(LAS bf16x8*)(p + 8192 + 1024);
    };
    auto tile = [&](int fb) {
#pragma unroll
      for (int qb = 0; qb < 2; ++qb) {
        f32x16 s = __builtin_amdgcn_mfma_f32_32x32x16_bf16(kf[fb][0], qf[qb][0], negm[qb], 0, 0, 0);
        s = __builtin_amdgcn_mfma_f32_32x32x16_bf16(kf[fb][1], qf[qb][1], s, 0, 0, 0);
        u32x4 p0, p1;
#pragma unroll
        for (int e = 0; e < 4; ++e) {
          p0[e] = pk2(__builtin_amdgcn_exp2f(s[2 * e]), __builtin_amdgcn_exp2f(s[2 * e + 1]));
          p1[e] = pk2(__builtin_amdgcn_exp2f(s[8 + 2 * e]), __builtin_amdgcn_exp2f(s[9 + 2 * e]));
        }
        const bf16x8 b0 = __builtin_bit_cast(bf16x8, p0), b1 = __builtin_bit_cast(bf16x8, p1);
        o[qb] = __builtin_amdgcn_mfma_f32_32x32x16_bf16(vf[fb][0], b0, o[qb], 0, 0, 0);
        l[qb] = __builtin_amdgcn_mfma_f32_16x16x32_bf16(sel, b0, l[qb], 0, 0, 0);
        o[qb] = __builtin_amdgcn_mfma_f32_32x32x16_bf16(vf[fb][1], b1, o[qb], 0, 0, 0);
        l[qb] = __builtin_amdgcn_mfma_f32_16x16x32_bf16(sel, b1, l[qb], 0, 0, 0);
      }
    };
    rd(0, 0, 0);
#pragma unroll 1
    for (int st = 0; st < 8; ++st) {
      const int slot = st & 3;
      rd(1, slot, 1); tile(0);
      rd(0, slot, 2); tile(1);
      rd(1, slot, 3); tile(0);
      WAIT_L(0);
      if (st <= 4) WAIT_V(8); else WAIT_V(0);
      BAR();
      if (st < 4) issue(st + 4);
      rd(0, (st + 1) & 3, 0);
      tile(1);
    }
    WAIT_L(0);
    bool bad = false;
#pragma unroll
    for (int qb = 0; qb < 2; ++qb) {
      const float a = __shfl(l[qb][0], lane & 15), b = __shfl(l[qb][1], lane & 15);
      lrow[qb] = (lane & 16) ? b : a;
      bad |= !(lrow[qb] <= 1.152921504606847e18f);
    }
    if (attempt == 1) break;
    if (!__syncthreads_or(bad)) break;
  }
  __syncthreads();
  {
    const int oq = half ^ 1;
    LAS char* mine = lds + (qw * 2 + oq) * 8192;
    const f32x16 ox = half ? o[0] : o[1];
#pragma unroll
    for (int i = 0; i < 4; ++i) {
      f32x4 t; t[0] = ox[4 * i]; t[1] = ox[4 * i + 1]; t[2] = ox[4 * i + 2]; t[3] = ox[4 * i + 3];
      *(LAS f32x4*)(mine + i * 1024 + lane * 16) = t;
    }
    f32x2 ml; ml[0] = half ? mrow[0] : mrow[1]; ml[1] = half ? lrow[0] : lrow[1];
    *(LAS f32x2*)(mine + 4096 + lane * 8) = ml;
  }
  __syncthreads();
  {
    LAS char* oth = lds + (qw * 2 + half) * 8192;
    const f32x16 om = half ? o[1] : o[0];
    const float m1 = half ? mrow[1] : mrow[0], l1 = half ? lrow[1] : lrow[0];
    const f32x2 ml = *(LAS f32x2*)(oth + 4096 + lane * 8);
    const float m = fmaxf(m1, ml[0]);
    const float a1 = __builtin_amdgcn_exp2f(m1 - m), a2 = __builtin_amdgcn_exp2f(ml[0] - m);
    const int mb = mb0 + half;
    const float g = *(const float*)(ws + WS_ST1 + (size_t)(mb * 32 + perm23(lane & 31)) * 16 + 8);
    const float inv = g / (l1 * a1 + ml[1] * a2);
    const float c1 = a1 * inv, c2 = a2 * inv;
    float r[16];
#pragma unroll
    for (int i = 0; i < 4; ++i) {
      const f32x4 t = *(LAS f32x4*)(oth + i * 1024 + lane * 16);
#pragma unroll
      for (int e = 0; e < 4; ++e) r[4 * i + e] = om[4 * i + e] * c1 + t[e] * c2;
    }
    char* dst = ws + WS_AC + ((size_t)mb * 288 + head * 2) * 1024 + lane * 16;
#pragma unroll
    for (int s = 0; s < 2; ++s) {
      u32x4 w;
#pragma unroll
      for (int e = 0; e < 4; ++e) w[e] = pk2(r[8 * s + 2 * e], r[8 * s + 2 * e + 1]);
      *(u32x4*)(dst + s * 1024) = w;
    }
  }
}

__global__ __launch_bounds__(256) void k_w2(const char* __restrict__ x, const char* __restrict__ w2, char* __restrict__ ws) {
  extern __shared__ __attribute__((aligned(16))) char smem[];
  LAS char* lds = (LAS char*)smem;
  const int bid = blockIdx.x, xcd = bid & 7, idx = bid >> 3;
  const int pm = (xcd >> 1) * 4 + (idx & 3), pn = (xcd & 1) * 8 + (idx >> 2);
  const int lane = threadIdx.x & 63, wid = __builtin_amdgcn_readfirstlane(threadIdx.x >> 6), wm = wid >> 1, wn = wid & 1;
  f32x16 acc[2][1] = {};
  float cs[1] = {0.f};
  const char* bp = w2 + pn * 128;
  Gemm<128, 64, 2, 2, 4, 1, true, false>::run(lds, ws + WS_HS + (size_t)pm * 4 * 65536, 65536, bp, bp, 1 << 20, 2048, 16, acc, cs);
  BAR();
  LAS f32x2* part = (LAS f32x2*)lds;
#pragma unroll
  for (int mi = 0; mi < 2; ++mi) {
    const int ml = wm * 64 + mi * 32 + perm23(lane & 31), m = pm * 128 + ml;
    const int n0 = pn * 64 + wn * 32;
    float v[16];
    u32x4 o[2];
#pragma unroll
    for (int s = 0; s < 2; ++s) {
      const u32x4 xv = *(const u32x4*)(x + (size_t)m * 2048 + (n0 + s * 16 + (lane >> 5) * 8) * 2);
#pragma unroll
      for (int e = 0; e < 4; ++e) {
        o[s][e] = pk2(bflo(xv[e]) + acc[mi][0][8 * s + 2 * e], bfhi(xv[e]) + acc[mi][0][8 * s + 2 * e + 1]);
        v[8 * s + 2 * e] = bflo(o[s][e]); v[8 * s + 2 * e + 1] = bfhi(o[s][e]);
      }
      *(u32x4*)(ws + WS_X1 + ((size_t)(m >> 5) * 64 + (n0 >> 4) + s) * 1024 + lane * 16) = o[s];
    }
    float sm = 0.f;
#pragma unroll
    for (int e = 0; e < 16; ++e) sm += v[e];
    sm += __shfl_xor(sm, 32);
    const float mean = sm * (1.0f / 32.0f);
    float q = 0.f;
#pragma unroll
    for (int e = 0; e < 16; ++e) { const float t = v[e] - mean; q += t * t; }
    q += __shfl_xor(q, 32);
    if (lane < 32) { f32x2 t; t[0] = mean; t[1] = q; part[ml * 2 + wn] = t; }
  }
  WAIT_L(0); BAR();
  if (threadIdx.x < 128) {
    const f32x2 a = part[threadIdx.x * 2], b = part[threadIdx.x * 2 + 1];
    const float d = a[0] - b[0];
    f32x2 t; t[0] = 0.5f * (a[0] + b[0]); t[1] = a[1] + b[1] + 16.0f * d * d;
    *(f32x2*)(ws + WS_ST2 + ((size_t)(pm * 128 + threadIdx.x) * 16 + pn) * 8) = t;
  }
}

__global__ __launch_bounds__(512) void k_w3(const char* __restrict__ w3, char* __restrict__ ws) {
  extern __shared__ __attribute__((aligned(16))) char smem[];
  LAS char* lds = (LAS char*)smem;
  const int bid = blockIdx.x, xcd = bid & 7, idx = bid >> 3;
  const int pm = (xcd & 1) * 4 + (idx & 3), pn = (xcd >> 1) * 7 + (idx >> 2);
  const int lane = threadIdx.x & 63, wid = __builtin_amdgcn_readfirstlane(threadIdx.x >> 6), wm = wid >> 1, wn = wid & 1;
  f32x16 acc[2][2] = {};
  float cs[2] = {0.f, 0.f};
  const char* bp = w3 + pn * 256;
  Gemm<256, 128, 4, 2, 3, 1, true, true>::run(lds, ws + WS_X1 + (size_t)pm * 8 * 65536, 65536, bp, bp, 1 << 20, 7168, 16, acc, cs);
  BAR();
  colsum_reduce<128, 4, 2, 2>(lds, cs, wm, wn, lane);
#pragma unroll
  for (int mi = 0; mi < 2; ++mi) {
    const int m = pm * 256 + wm * 64 + mi * 32 + perm23(lane & 31);
    const f32x4* sp = (const f32x4*)(ws + WS_ST2 + (size_t)m * 128 + (lane >> 5) * 64);
    f32x4 p[4];
#pragma unroll
    for (int i = 0; i < 4; ++i) p[i] = sp[i];
    float sm = 0.f, q = 0.f;
#pragma unroll
    for (int i = 0; i < 4; ++i) { sm += p[i][0] + p[i][2]; q += p[i][1] + p[i][3]; }
    sm += __shfl_xor(sm, 32);
    const float mu = sm * (1.0f / 16.0f);
    float dd = 0.f;
#pragma unroll
    for (int i = 0; i < 4; ++i) { const float a = p[i][0] - mu, b = p[i][2] - mu; dd += a * a + b * b; }
    q += 64.0f * dd;
    q += __shfl_xor(q, 32);
    const float rs = rsqrtf(q * (1.0f / 1024.0f) + 1e-5f), nmu = -mu;
    const float og = *(const float*)(ws + WS_ST1 + (size_t)m * 16 + 12);
    char* dst = ws + WS_AC + ((size_t)(m >> 5) * 288 + 64 + pn * 8 + wn * 4) * 1024 + lane * 16;
#pragma unroll
    for (int ni = 0; ni < 2; ++ni)
#pragma unroll
      for (int s = 0; s < 2; ++s) {
        const int nl = wn * 64 + ni * 32 + s * 16 + (lane >> 5) * 8;
        const f32x4 c0 = *(LAS f32x4*)(lds + nl * 4), c1 = *(LAS f32x4*)(lds + nl * 4 + 16);
        float hv[8];
#pragma unroll
        for (int e = 0; e < 8; ++e) {
          const float c = e < 4 ? c0[e & 3] : c1[e & 3];
          hv[e] = og * gelu_tanh(rs * __builtin_fmaf(nmu, c, acc[mi][ni][8 * s + e]));
        }
        u32x4 o;
#pragma unroll
        for (int e = 0; e < 4; ++e) o[e] = pk2(hv[2 * e], hv[2 * e + 1]);
        *(u32x4*)(dst + (ni * 2 + s) * 1024) = o;
      }
  }
}

__global__ __launch_bounds__(256) void k_final(const char* __restrict__ wo, const char* __restrict__ w4,
                                               const char* __restrict__ ws, char* __restrict__ out) {
  extern __shared__ __attribute__((aligned(16))) char smem[];
  LAS char* lds = (LAS char*)smem;
  const int bid = blockIdx.x, xcd = bid & 7, idx = bid >> 3;
  const int pm = (xcd >> 1) * 4 + (idx & 3), pn = (xcd & 1) * 8 + (idx >> 2);
  const int lane = threadIdx.x & 63, wid = __builtin_amdgcn_readfirstlane(threadIdx.x >> 6), wm = wid >> 1, wn = wid & 1;
  f32x16 acc[2][1] = {};
  float cs[1] = {0.f};
  Gemm<128, 64, 2, 2, 6, 1, true, false>::run(lds, ws + WS_AC + (size_t)pm * 4 * 288 * 1024, 288 * 1024, wo + pn * 128,
                                              w4 + pn * 128 - (size_t)1024 * 2048, 16, 2048, 72, acc, cs);
#pragma unroll
  for (int mi = 0; mi < 2; ++mi) {
    const int m = pm * 128 + wm * 64 + mi * 32 + perm23(lane & 31);
    const int n0 = pn * 64 + wn * 32;
    const float og = *(const float*)(ws + WS_ST1 + (size_t)m * 16 + 12);
#pragma unroll
    for (int s = 0; s < 2; ++s) {
      const u32x4 xv = *(const u32x4*)(ws + WS_X1 + ((size_t)(m >> 5) * 64 + (n0 >> 4) + s) * 1024 + lane * 16);
      u32x4 o;
#pragma unroll
      for (int e = 0; e < 4; ++e)
        o[e] = pk2(__builtin_fmaf(og, bflo(xv[e]), acc[mi][0][8 * s + 2 * e]), __builtin_fmaf(og, bfhi(xv[e]), acc[mi][0][8 * s + 2 * e + 1]));
      *(u32x4*)(out + (size_t)m * 2048 + (n0 + s * 16 + (lane >> 5) * 8) * 2) = o;
    }
  }
}

extern "C" void kernel_launch(void* const* d_in, const int* in_sizes, int n_in, void* d_out, int out_size, void* d_ws,
                              size_t ws_size, hipStream_t stream) {
  const char* x = (const char*)d_in[0];
  const char* wqkv = (const char*)d_in[1];
  const char* wo = (const char*)d_in[2];
  const char* w1 = (const char*)d_in[4];
  const char* w2 = (const char*)d_in[5];
  const char* w3 = (const char*)d_in[6];
  const char* w4 = (const char*)d_in[7];
  const char* gw = (const char*)d_in[8];
  char* ws = (char*)d_ws;
  (void)hipFuncSetAttribute((const void*)k_qkv_w1, hipFuncAttributeMaxDynamicSharedMemorySize, 147456);
  (void)hipFuncSetAttribute((const void*)k_attn, hipFuncAttributeMaxDynamicSharedMemorySize, 131072);
  (void)hipFuncSetAttribute((const void*)k_w2, hipFuncAttributeMaxDynamicSharedMemorySize, 98304);
  (void)hipFuncSetAttribute((const void*)k_w3, hipFuncAttributeMaxDynamicSharedMemorySize, 147456);
  (void)hipFuncSetAttribute((const void*)k_final, hipFuncAttributeMaxDynamicSharedMemorySize, 147456);
  k_prep<<<512, 256, 0, stream>>>(x, gw, ws);
  k_qkv_w1<<<256, 512, 147456, stream>>>(x, wqkv, w1, ws);
  k_attn<<<256, 512, 131072, stream>>>(ws);
  k_w2<<<256, 256, 98304, stream>>>(x, w2, ws);
  k_w3<<<224, 512, 147456, stream>>>(w3, ws);
  k_final<<<256, 256, 147456, stream>>>(wo, w4, ws, (char*)d_out);
}
```

```cpp
#include <hip/hip_runtime.h>
#include <stdint.h>

typedef __attribute__((ext_vector_type(8))) __bf16 bf16x8;
typedef __attribute__((ext_vector_type(2))) __bf16 bf16x2;
typedef __attribute__((ext_vector_type(4))) short s16x4;
typedef __attribute__((ext_vector_type(16))) float f32x16;
typedef __attribute__((ext_vector_type(4))) float f32x4;
typedef __attribute__((ext_vector_type(4))) unsigned u32x4;
typedef __attribute__((ext_vector_type(2))) unsigned u32x2;
typedef __amdgpu_buffer_rsrc_t rsrc_t;
#define LAS __attribute__((address_space(3)))
#define DEV __device__ __forceinline__

#define WAIT_V(n) asm volatile("s_waitcnt vmcnt(" #n ")" ::: "memory")
#define LBAR() asm volatile("s_waitcnt lgkmcnt(0)\n\ts_barrier" ::: "memory")
#define SCHED() __builtin_amdgcn_sched_barrier(0)
#define MFMA32(a, b, c) __builtin_amdgcn_mfma_f32_32x32x16_bf16(a, b, c, 0, 0, 0)
#define MFMA16(a, b, c) __builtin_amdgcn_mfma_f32_16x16x32_bf16(a, b, c, 0, 0, 0)

constexpr unsigned MB = 1024u * 1024u;
constexpr unsigned WS_Q = 0, WS_K = 4 * MB, WS_V = 8 * MB;
constexpr unsigned WS_HS = 12 * MB, WS_XN = 16 * MB, WS_O = 20 * MB, WS_H = 24 * MB, WS_P = 38 * MB;
constexpr unsigned WS_ST1 = 46 * MB, WS_ST2 = 47 * MB, WS_FL = 48 * MB, WS_END = 49 * MB;
constexpr unsigned FL_E = WS_FL, FL_1 = WS_FL + 4096, FL_2 = FL_1 + 8192, FL_4 = FL_2 + 8192, FL_5 = FL_4 + 8192,
                   FL_6 = FL_5 + 8192, FL_7 = FL_6 + 8192;
constexpr int SLOT = 49152, SPARE = 3 * SLOT, LDS_BYTES = 163840;
constexpr int ATT1 = 4;
constexpr float GS = 0.46866869f, GA = 4.9122295f, GSI = 1.0f / GS;

#define STAMP(i)

DEV int perm23(int i) { return (i & ~12) | ((i & 4) << 1) | ((i & 8) >> 1); }
DEV unsigned pk2(float a, float b) {
  bf16x2 v; v[0] = (__bf16)a; v[1] = (__bf16)b;
  return __builtin_bit_cast(unsigned, v);
}
DEV float bflo(unsigned u) { return __builtin_bit_cast(float, u << 16); }
DEV float bfhi(unsigned u) { return __builtin_bit_cast(float, u & 0xffff0000u); }
DEV float rbf(float a) { return (float)(__bf16)a; }
DEV float asf(unsigned u) { return __builtin_bit_cast(float, u); }
DEV unsigned asu(float f) { return __builtin_bit_cast(unsigned, f); }
DEV float gelu_s(float v) {
  const float e = __builtin_amdgcn_exp2f(-v * __builtin_fmaf(v, v, GA));
  return v * __builtin_amdgcn_rcpf(1.0f + e);
}
DEV float xhalf_sum(float v) {
  const auto r = __builtin_amdgcn_permlane32_swap(asu(v), asu(v), false, false);
  return asf(r[0]) + asf(r[1]);
}
DEV float xhalf_max(float v) {
  const auto r = __builtin_amdgcn_permlane32_swap(asu(v), asu(v), false, false);
  return fmaxf(asf(r[0]), asf(r[1]));
}
DEV void st16(rsrc_t r, unsigned off, u32x4 v) { __builtin_amdgcn_raw_buffer_store_b128(v, r, off, 0, 16); }
DEV u32x4 ld16(rsrc_t r, unsigned off) { return __builtin_amdgcn_raw_buffer_load_b128(r, off, 0, 16); }
DEV u32x2 ld8(rsrc_t r, unsigned off) { return __builtin_amdgcn_raw_buffer_load_b64(r, off, 0, 16); }
#define INV 0
#define SCL " sc1"
#define P2 3
#define P6 2
#define P7 2
#define SPIN 20
constexpr int SPIN_CAP = 1 << SPIN;
DEV void spin_pause() { __builtin_amdgcn_s_sleep(4); asm volatile("" ::: "memory"); }
DEV void publish(rsrc_t r, unsigned off, unsigned tag, int lane) {
  WAIT_V(0);
  if (lane == 0) __builtin_amdgcn_raw_buffer_store_b32(tag, r, off, 0, 16);
}
DEV void poll(rsrc_t r, unsigned off, unsigned tag) {
#pragma unroll 1
  for (int it = 0; it < SPIN_CAP; ++it) {
    const u32x4 v = ld16(r, off);
    if (__all(v[0] == tag && v[1] == tag && v[2] == tag && v[3] == tag)) break;
    spin_pause();
  }
}

DEV u32x4 peek(rsrc_t r, unsigned off) {
  u32x4 v;
  asm volatile("buffer_load_dwordx4 %0, %1, %2, 0 offen sc1" : "=v"(v) : "v"(off), "s"(r) : "memory");
  return v;
}
DEV void check(u32x4 v, rsrc_t r, unsigned off, unsigned tag) {
  asm volatile("" : "+v"(v)::"memory");
  if (!__all(v[0] == tag && v[1] == tag && v[2] == tag && v[3] == tag)) poll(r, off, tag);
}

#define GLDS_ "global_load_lds_dwordx4 "
DEV void glds1(const char* sb, unsigned l, unsigned v) {
  asm volatile("s_mov_b32 m0, %1\n\ts_nop 0\n\t" GLDS_ "%2, %0" ::"s"(sb), "s"(l), "v"(v) : "memory");
}
DEV void glds2(const char* sb, unsigned l, unsigned v) {
  asm volatile("s_mov_b32 m0, %1\n\ts_nop 0\n\t" GLDS_ "%2, %0" SCL "\n\t" GLDS_ "%2, %0 offset:1024" SCL ::"s"(sb), "s"(l), "v"(v) : "memory");
}
DEV void glds4(const char* sb, unsigned l, unsigned v) {
  asm volatile("s_mov_b32 m0, %1\n\ts_nop 0\n\t" GLDS_ "%2, %0" SCL "\n\t" GLDS_ "%2, %0 offset:1024" SCL "\n\t" GLDS_ "%2, %0 offset:2048" SCL "\n\t" GLDS_ "%2, %0 offset:3072" SCL
               ::"s"(sb), "s"(l), "v"(v) : "memory");
}
DEV void touch(const char* sb, unsigned l, unsigned v) {
  asm volatile("s_mov_b32 m0, %1\n\ts_nop 0\n\tglobal_load_lds_dword %2, %0" ::"s"(sb), "s"(l), "v"(v) : "memory");
}
template <int N> DEV void vwait() { asm volatile("s_waitcnt vmcnt(%0) lgkmcnt(0)" ::"n"(N) : "memory"); }
template <int N> DEV void vbar_() { asm volatile("s_waitcnt vmcnt(%0) lgkmcnt(0)\n\ts_barrier" ::"n"(N) : "memory"); }
#define VBAR(N) vbar_<N>()
DEV int next_slot(int sb) { return sb == 2 * SLOT ? 0 : sb + SLOT; }
DEV bf16x8 cat(u32x2 a, u32x2 b) {
  u32x4 c; c[0] = a[0]; c[1] = a[1]; c[2] = b[0]; c[3] = b[1];
  return __builtin_bit_cast(bf16x8, c);
}
DEV u32x2 rd_tr(LAS char* p) { return __builtin_bit_cast(u32x2, __builtin_amdgcn_ds_read_tr16_b64_v4i16((LAS s16x4*)p)); }
template <int RB> DEV int wfrag_off(int lane, int c0) {
  const int h = lane >> 5, blk = (lane >> 4) & 1, q = (lane >> 2) & 3, p = lane & 3, pp = ((p & 1) << 1) | (p >> 1);
  const int ch = c0 + blk * 2 + (pp >> 1), sw = RB == 128 ? ((q >> 1) << 2) : 0;
  return (8 * h + q) * RB + ((ch ^ sw) << 4) + (pp & 1) * 8;
}

template <int NT, int G, int NG, int AMODE, int KSA, int RB, int NSTR, int VMASK>
struct Gemm {
  static constexpr int M = G * NT, RK = 1 + 2 * NT;
  LAS char* lds;
  int ra, rb;
  bf16x8 xf[2][G];
  u32x2 wl[2][G][NT], wh[2][G][NT];
  DEV Gemm(LAS char* l, int lane, int wm, int boff, int c0) : lds(l) {
    if (AMODE == 0) {
      const int row = wm * 32 + perm23(lane & 31);
      ra = row * 128 + (((lane >> 5) ^ ((row >> 1) & 7)) << 4);
    } else {
      ra = wm * KSA * 1024 + lane * 16;
    }
    rb = boff + wfrag_off<RB>(lane, c0);
  }
  DEV void rd(int r, int fb, int sb, int s0) {
    const int g = r / RK, i = r % RK, s = s0 + g;
    if (i == 2) {
      if (AMODE == 0) xf[fb][g] = *(LAS bf16x8*)(lds + sb + (ra ^ ((s & 3) << 5)) + (s >> 2) * 16384);
      else xf[fb][g] = *(LAS bf16x8*)(lds + sb + ra + s * 1024);
    } else {
      const int j = i < 2 ? i : i - 1, ni = j >> 1;
      LAS char* p = lds + sb + rb + ni * NSTR + s * 16 * RB;
      if (j & 1) wh[fb][g][ni] = rd_tr(p + 4 * RB); else wl[fb][g][ni] = rd_tr(p);
    }
  }
  DEV void first(int sb) {
#pragma unroll
    for (int r = 0; r < G * RK; ++r) rd(r, 0, sb, 0);
  }
  template <int GC, int GN, int G0, int G1, bool BAR>
  DEV void group(f32x16 (&acc)[NT], int fb, int sb, int s0) {
    constexpr int NR = GN * RK;
#pragma unroll
    for (int i = 0; i < GC * NT; ++i) {
      const int g = i / NT, ni = i % NT;
      const bf16x8 w = cat(wl[fb][g][ni], wh[fb][g][ni]);
      if ((VMASK >> ni) & 1) acc[ni] = MFMA32(xf[fb][g], w, acc[ni]);
      else acc[ni] = MFMA32(w, xf[fb][g], acc[ni]);
      SCHED();
      if (BAR && i == 0) { LBAR(); SCHED(); }
      if (i >= G0 && i < G1) {
        constexpr int GD = G1 > G0 ? G1 - G0 : 1;
        const int r0 = NR * (i - G0) / GD, r1 = NR * (i - G0 + 1) / GD;
#pragma unroll
        for (int r = 0; r < NR; ++r) if (r >= r0 && r < r1) rd(r, fb ^ 1, sb, s0);
        SCHED();
      }
    }
  }
  template <bool LAST, int GL>
  DEV void step(f32x16 (&acc)[NT], int sb, int sbn) {
    constexpr int PRE = M >= 6 ? M - 2 : M - 1;
#pragma unroll
    for (int gi = 0; gi < NG; ++gi) {
      if (gi < NG - 2) group<G, G, 0, M - 1, false>(acc, gi & 1, sb, (gi + 1) * G);
      else if (gi == NG - 2) group<G, GL, 0, PRE, false>(acc, gi & 1, sb, (gi + 1) * G);
      else if (LAST) group<GL, 0, 0, 0, true>(acc, gi & 1, sbn, 0);
      else group<G, G, (M >= 6 ? 1 : 0), M - 1, true>(acc, gi & 1, sbn, 0);
    }
  }
  template <class H>
  DEV void run(f32x16 (&acc)[NT], int& sb, int k0, int k1, H hook) {
#pragma unroll 1
    for (int kt = k0; kt < k1; ++kt) {
      const int sbn = next_slot(sb);
      step<false, G>(acc, sb, sbn);
      sb = sbn;
      hook(kt);
    }
  }
  template <int GL>
  DEV void last(f32x16 (&acc)[NT], int& sb) {
    const int sbn = next_slot(sb);
    step<true, GL>(acc, sb, sbn);
    sb = sbn;
  }
};

DEV u32x4 pack8(const f32x16& a, int s, float sc) {
  u32x4 r;
#pragma unroll
  for (int e = 0; e < 4; ++e) r[e] = pk2(a[8 * s + 2 * e] * sc, a[8 * s + 2 * e + 1] * sc);
  return r;
}
template <int C> DEV float dpp_add(float v) { return v + asf(__builtin_amdgcn_update_dpp(0, asu(v), C, 0xf, 0xf, true)); }
DEV float wave_sum(float v) {
  v = dpp_add<0x140>(dpp_add<0x141>(dpp_add<0x4E>(dpp_add<0xB1>(v))));
  const auto r = __builtin_amdgcn_permlane16_swap(asu(v), asu(v), false, false);
  return xhalf_sum(asf(r[0]) + asf(r[1]));
}

struct Att {
  bf16x8 q0, q1, sel;
  f32x16 o;
  f32x4 l;
  float m;
};
template <bool FIRST, class H>
DEV void attend(Att& a, LAS char* lds, int& sb, int nsteps, int lane, int wid, H hook) {
  const int lo = lane * 16;
  bf16x8 k0, k1, v0[2], v1[2];
  f32x16 s[2], negm;
  u32x4 p0, p1 = {0u, 0u, 0u, 0u};
  {
    const unsigned one2 = (((lane >> 2) & 1) == ((lane >> 4) & 1)) ? 0x3f803f80u : 0u;
    const u32x4 t = {one2, one2, one2, one2};
    a.sel = __builtin_bit_cast(bf16x8, t);
  }
  {
    LAS char* p = lds + sb + lo;
    k0 = *(LAS bf16x8*)p; k1 = *(LAS bf16x8*)(p + 1024);
    if (FIRST) { a.q0 = *(LAS bf16x8*)(p + 32768 + wid * 2048); a.q1 = *(LAS bf16x8*)(p + 32768 + wid * 2048 + 1024); }
    v0[0] = *(LAS bf16x8*)(p + 16384); v1[0] = *(LAS bf16x8*)(p + 16384 + 1024);
    v1[1] = __builtin_bit_cast(bf16x8, p1);
    if (FIRST) {
      f32x16 t = {};
      t = MFMA32(k0, a.q0, t);
      t = MFMA32(k1, a.q1, t);
      float m = t[0];
#pragma unroll
      for (int e = 1; e < 16; ++e) m = fmaxf(m, t[e]);
      a.m = xhalf_max(m);
    }
#pragma unroll
    for (int e = 0; e < 16; ++e) negm[e] = -a.m;
    s[0] = MFMA32(k0, a.q0, negm);
    s[0] = MFMA32(k1, a.q1, s[0]);
    SCHED();
    k0 = *(LAS bf16x8*)(p + 2048); k1 = *(LAS bf16x8*)(p + 3072);
    SCHED();
  }
#define EX(i) e[i] = __builtin_amdgcn_exp2f(s[c][i])
#pragma unroll 1
  for (int st = 0; st < nsteps; ++st) {
    const int sbn = next_slot(sb);
    LAS char* pc = lds + sb + lo;
    LAS char* pn = lds + sbn + lo;
#pragma unroll
    for (int t = 0; t < 8; ++t) {
      const int c = t & 1;
      float e[16];
      a.o = MFMA32(v1[c ^ 1], __builtin_bit_cast(bf16x8, p1), a.o);
      SCHED();
      {
        LAS char* p = (t < 7 ? pc + (t + 1) * 2048 : pn) + 16384;
        v0[c ^ 1] = *(LAS bf16x8*)p; v1[c ^ 1] = *(LAS bf16x8*)(p + 1024);
      }
      EX(0); EX(1); EX(2); EX(3);
      SCHED();
      a.l = MFMA16(a.sel, __builtin_bit_cast(bf16x8, p1), a.l);
      SCHED();
      p0[0] = pk2(e[0], e[1]); p0[1] = pk2(e[2], e[3]); EX(4); EX(5);
      SCHED();
      s[c ^ 1] = MFMA32(k0, a.q0, negm);
      SCHED();
      EX(6); EX(7); p0[2] = pk2(e[4], e[5]); p0[3] = pk2(e[6], e[7]);
      SCHED();
      s[c ^ 1] = MFMA32(k1, a.q1, s[c ^ 1]);
      SCHED();
      if (t == 6) { LBAR(); SCHED(); }
      {
        LAS char* p = t < 6 ? pc + (t + 2) * 2048 : pn + (t - 6) * 2048;
        k0 = *(LAS bf16x8*)p; k1 = *(LAS bf16x8*)(p + 1024);
      }
      EX(8); EX(9); EX(10); EX(11);
      SCHED();
      a.o = MFMA32(v0[c], __builtin_bit_cast(bf16x8, p0), a.o);
      SCHED();
      p1[0] = pk2(e[8], e[9]); p1[1] = pk2(e[10], e[11]); EX(12); EX(13);
      SCHED();
      a.l = MFMA16(a.sel, __builtin_bit_cast(bf16x8, p0), a.l);
      SCHED();
      EX(14); EX(15); p1[2] = pk2(e[12], e[13]); p1[3] = pk2(e[14], e[15]);
      SCHED();
      if (t == 2) hook(st);
    }
    sb = sbn;
  }
#undef EX
  a.o = MFMA32(v1[1], __builtin_bit_cast(bf16x8, p1), a.o);
  a.l = MFMA16(a.sel, __builtin_bit_cast(bf16x8, p1), a.l);
}
DEV void attend_exact(Att& a, rsrc_t rw, int head, int lane) {
  const unsigned kb = WS_K + head * 131072 + lane * 16;
  float m = -INFINITY;
#pragma unroll 1
  for (int t = 0; t < 64; ++t) {
    const bf16x8 k0 = __builtin_bit_cast(bf16x8, ld16(rw, kb + t * 2048)), k1 = __builtin_bit_cast(bf16x8, ld16(rw, kb + t * 2048 + 1024));
    f32x16 s = {};
    s = MFMA32(k0, a.q0, s);
    s = MFMA32(k1, a.q1, s);
#pragma unroll
    for (int e = 0; e < 16; ++e) m = fmaxf(m, s[e]);
  }
  m = xhalf_max(m);
  a.o = (f32x16){};
  a.l = (f32x4){};
#pragma unroll 1
  for (int t = 0; t < 64; ++t) {
    const bf16x8 k0 = __builtin_bit_cast(bf16x8, ld16(rw, kb + t * 2048)), k1 = __builtin_bit_cast(bf16x8, ld16(rw, kb + t * 2048 + 1024));
    const bf16x8 v0 = __builtin_bit_cast(bf16x8, ld16(rw, kb + 4 * MB + t * 2048)), v1 = __builtin_bit_cast(bf16x8, ld16(rw, kb + 4 * MB + t * 2048 + 1024));
    f32x16 s = {};
    s = MFMA32(k0, a.q0, s);
    s = MFMA32(k1, a.q1, s);
    u32x4 p0, p1;
#pragma unroll
    for (int e = 0; e < 4; ++e) {
      p0[e] = pk2(__builtin_amdgcn_exp2f(s[2 * e] - m), __builtin_amdgcn_exp2f(s[2 * e + 1] - m));
      p1[e] = pk2(__builtin_amdgcn_exp2f(s[8 + 2 * e] - m), __builtin_amdgcn_exp2f(s[9 + 2 * e] - m));
    }
    a.o = MFMA32(v0, __builtin_bit_cast(bf16x8, p0), a.o);
    a.l = MFMA16(a.sel, __builtin_bit_cast(bf16x8, p0), a.l);
    a.o = MFMA32(v1, __builtin_bit_cast(bf16x8, p1), a.o);
    a.l = MFMA16(a.sel, __builtin_bit_cast(bf16x8, p1), a.l);
  }
}

template <int NT>
DEV void w3_phase(LAS char* lds, int& sb, rsrc_t rw, int lane, int wm, int wn, int pm, int pn, char* ws) {
  f32x16 acc[NT] = {};
  Gemm<NT, 1, 4, 1, 4, 448, 64, 0> g(lds, lane, wm, 16384, wn * 16);
  g.first(sb);
  g.run(acc, sb, 0, 15, [](int) {});
  g.template last<1>(acc, sb);
  STAMP(9);
  const unsigned dst = WS_H + ((pm * 4 + wm) * 224 + pn * 14 + wn * 8) * 1024 + lane * 16;
#pragma unroll
  for (int ni = 0; ni < NT; ++ni)
#pragma unroll
    for (int s = 0; s < 2; ++s) {
      u32x4 o;
#pragma unroll
      for (int e = 0; e < 4; ++e) o[e] = pk2(gelu_s(acc[ni][8 * s + 2 * e]), gelu_s(acc[ni][8 * s + 2 * e + 1]));
      st16(rw, dst + (ni * 2 + s) * 1024, o);
    }
}

__global__ __launch_bounds__(768) void k_mega(const char* __restrict__ x, const char* __restrict__ wqkv, const char* __restrict__ wo,
                                              const char* __restrict__ w1, const char* __restrict__ w2, const char* __restrict__ w3,
                                              const char* __restrict__ w4, const char* __restrict__ gw, char* __restrict__ ws,
                                              char* __restrict__ out) {
  extern __shared__ __attribute__((aligned(16))) char smem[];
  LAS char* lds = (LAS char*)smem;
  const unsigned ldsa = (unsigned)(uintptr_t)lds;
  const int bid = blockIdx.x, xcd = bid & 7, idx = bid >> 3;
  const int pm = (xcd >> 1) * 4 + (idx & 3), pn = (xcd & 1) * 8 + (idx >> 2);
  const int head = xcd * 4 + (idx & 3), qg = idx >> 2;
  int lane = threadIdx.x & 63;
  const int wid = __builtin_amdgcn_readfirstlane(threadIdx.x >> 6);
  const rsrc_t rw = __builtin_amdgcn_make_buffer_rsrc(ws, 0, WS_END, 0x00020000);
  STAMP(0);

  if (wid >= 8) {
    const int lw = wid - 8;
    __builtin_amdgcn_s_setprio(3);
    const unsigned l16 = lane * 16;
    const unsigned vx0 = (lane >> 3) * 2048 + (((lane & 7) ^ (lane >> 4)) << 4), vx1 = vx0 ^ 64;
    const unsigned vsw = ((lane & 7) ^ (((lane >> 4) & 1) << 2)) << 4;
    const unsigned vb2 = (lane >> 3) * 2048 + vsw, vb6 = (lane >> 3) * 6144 + vsw;
    int sb = 0;
    auto panel = [&](const char* src, unsigned bstr, unsigned l, unsigned v, int n) {
#pragma unroll
      for (int i = 0; i < 4; ++i) if (i < n) glds1(src + (size_t)i * 8 * bstr, l + i * 1024, v);
    };
    auto nb = [&](const char* w, int kt) {
      panel(w + (size_t)(kt * 128 + lw * 32) * 2048 + pn * 128, 2048, ldsa + sb + 32768 + lw * 4096, vb2, 4);
    };
    auto i_w1 = [&](int kt) {
      const char* a = x + (size_t)(pm * 128 + (lw & 1) * 64) * 2048 + kt * 256 + (lw >> 1) * 128;
#pragma unroll
      for (int i = 0; i < 8; ++i) glds1(a + i * 16384, ldsa + sb + lw * 8192 + i * 1024, (i & 1) ? vx1 : vx0);
      nb(w1, kt);
      sb = next_slot(sb);
    };
    auto i_nf = [&](unsigned src, const char* w, int kt) {
      const char* a = ws + src + (size_t)((pm * 4 + lw) * 64 + kt * 8) * 1024;
      glds4(a, ldsa + sb + lw * 8192, l16);
      glds4(a + 4096, ldsa + sb + lw * 8192 + 4096, l16);
      nb(w, kt);
      sb = next_slot(sb);
    };
    auto i_qkv = [&](int kt) {
      const char* a = x + (size_t)(pm * 128 + lw * 32) * 2048 + kt * 128;
#pragma unroll
      for (int i = 0; i < 4; ++i) glds1(a + i * 16384, ldsa + sb + lw * 4096 + i * 1024, (i & 1) ? vx1 : vx0);
      const char* b = wqkv + (size_t)(kt * 64 + lw * 16) * 6144 + pn * 128;
#pragma unroll
      for (int sg = 0; sg < 3; ++sg) panel(b + sg * 2048, 6144, ldsa + sb + 16384 + sg * 8192 + lw * 2048, vb6, 2);
      sb = next_slot(sb);
    };
    auto i_att = [&](int st) {
      const char* a = ws + WS_K + (size_t)(lw >> 1) * 4 * MB + head * 131072 + st * 16384 + (lw & 1) * 8192;
      glds4(a, ldsa + sb + lw * 8192, l16);
      glds4(a + 4096, ldsa + sb + lw * 8192 + 4096, l16);
      if (st == 0) glds4(ws + WS_Q + head * 131072 + qg * 16384 + lw * 4096, ldsa + sb + 32768 + lw * 4096, l16);
      sb = next_slot(sb);
    };
    unsigned ev;
    asm volatile("buffer_load_dword %0, off, %1, %2 sc1" : "=v"(ev) : "s"(rw), "s"(FL_E) : "memory");
    i_w1(0); i_w1(1);
    VBAR(12);
    asm volatile("" : "+v"(ev)::"memory");
    const unsigned tag = __builtin_amdgcn_readfirstlane(ev) + 1u;
    i_w1(2);
    {
      float cs = 0.f;
      const int rb = 32768 + (lw >> 1) * 8192 + wfrag_off<128>(lane, (lw & 1) * 4);
      const bf16x2 one = __builtin_bit_cast(bf16x2, 0x3f803f80u);
#pragma unroll 1
      for (int r = 3; r < 11; ++r) {
        u32x2 t[8];
#pragma unroll
        for (int i = 0; i < 8; ++i) t[i] = rd_tr(lds + sb + rb + (i >> 1) * 2048 + (i & 1) * 512);
#pragma unroll
        for (int i = 0; i < 16; ++i) { const unsigned u = t[i >> 1][i & 1]; cs = __builtin_amdgcn_fdot2_f32_bf16(__builtin_bit_cast(bf16x2, u), one, cs, false); }
        if (r == 10) {
          cs = xhalf_sum(cs);
          *(LAS float*)(lds + SPARE + lw * 128 + perm23(lane & 31) * 4) = cs;
        }
        if (r <= 8) VBAR(12); else VBAR(10);
        if (r < 8) i_w1(r); else i_qkv(r - 8);
      }
    }
#pragma unroll 1
    for (int kt = 3; kt < 15; ++kt) { VBAR(10); i_qkv(kt); }
    {
      const int mbw = min(max(pm * 4 - 1 + min(lane >> 3, 5), 0), 63);
      const unsigned fo = FL_1 + mbw * 128 + (lane & 7) * 16;
      VBAR(10);
      const u32x4 f = peek(rw, fo);
      i_qkv(15);
      VBAR(10);
      check(f, rw, fo, tag);
    }
    i_nf(WS_HS, w2, 0);
#pragma unroll 1
    for (int kt = 1; kt < 7; ++kt) { VBAR(12); i_nf(WS_HS, w2, kt); }
    {
      const unsigned fo = FL_2 + head * 256 + (lane & 15) * 16;
      VBAR(12);
      const u32x4 f = peek(rw, fo);
      i_nf(WS_HS, w2, 7);
      VBAR(12);
      check(f, rw, fo, tag);
    }
    i_att(0);
    VBAR(12); i_att(1);
    VBAR(8); i_att(2);
    VBAR(8); i_att(3);
    {
      const unsigned so = WS_ST2 + ((lane >> 5) * 16 * 2048 + (pm * 4 + lw) * 32 + (lane & 31)) * 16;
      u32x4 st[16];
#pragma unroll 1
      for (int it = 0; it < SPIN_CAP; ++it) {
        bool ok = true;
#pragma unroll
        for (int i = 0; i < 16; ++i) st[i] = ld16(rw, so + i * 32768);
#pragma unroll
        for (int i = 0; i < 16; ++i) ok = ok && st[i][1] == tag && st[i][3] == tag;
        if (__all(ok)) break;
        spin_pause();
      }
      float sm = 0.f, q = 0.f;
#pragma unroll
      for (int i = 0; i < 16; ++i) { sm += asf(st[i][0]); q += asf(st[i][2]); }
      const float mu = xhalf_sum(sm) * (1.0f / 32.0f);
      float dd = 0.f;
#pragma unroll
      for (int i = 0; i < 16; ++i) { const float d = asf(st[i][0]) - mu; dd += d * d; }
      q = xhalf_sum(q + 32.0f * dd);
      const float rs = rsqrtf(q * (1.0f / 1024.0f) + 1e-5f) * GS, nm = -mu * rs;
#pragma unroll
      for (int j = 0; j < 4; ++j) {
        const u32x4 v = *(LAS u32x4*)(lds + SPARE + (lw * 4 + j) * 1024 + l16);
        u32x4 o;
#pragma unroll
        for (int e = 0; e < 4; ++e) o[e] = pk2(__builtin_fmaf(bflo(v[e]), rs, nm), __builtin_fmaf(bfhi(v[e]), rs, nm));
        st16(rw, WS_XN + ((pm * 4 + lw) * 64 + pn * 4 + j) * 1024 + l16, o);
      }
      publish(rw, FL_4 + ((pm * 4 + lw) * 16 + pn) * 4, tag, lane);
    }
    static_assert(ATT1 == 4, "schedule");
    {
      unsigned v3[7];
#pragma unroll
      for (int i = 0; i < 7; ++i) { const int id = i * 64 + lane; v3[i] = (id / 28) * 7168 + (id % 28) * 16; }
      auto i_w3 = [&](int kt) {
        glds4(ws + WS_XN + (size_t)((pm * 4 + lw) * 64 + kt * 4) * 1024, ldsa + sb + lw * 4096, l16);
        const char* b = w3 + (size_t)(kt * 64 + lw * 16) * 7168 + pn * 448;
#pragma unroll
        for (int i = 0; i < 7; ++i) glds1(b, ldsa + sb + 16384 + lw * 7168 + i * 1024, v3[i]);
        sb = next_slot(sb);
      };
      VBAR(0);
      VBAR(0);
      poll(rw, FL_4 + pm * 256 + (lane & 15) * 16, tag);
      i_w3(0);
#pragma unroll 1
      for (int kt = 1; kt < 16; ++kt) { i_w3(kt); VBAR(11); }
    }
#pragma unroll 1
    for (int st = ATT1; st < 8; ++st) { i_att(st); VBAR(8); }
    {
      const int kh = pn >> 3;
      const char* a0 = ws + WS_H + (size_t)((pm * 4 + lw) * 224 + kh * 112) * 1024;
      const char* b0 = w4 + (size_t)kh * 1792 * 2048;
      auto i_w4 = [&](int kt) {
        glds4(a0 + kt * 6144, ldsa + sb + lw * 6144, l16);
        glds2(a0 + kt * 6144 + 4096, ldsa + sb + lw * 6144 + 4096, l16);
        const char* b = b0 + (size_t)(kt * 96 + lw * 24) * 2048;
        panel(b + pn * 128, 2048, ldsa + sb + 24576 + lw * 3072, vb2, 3);
        panel(b + (pn ^ 8) * 128, 2048, ldsa + sb + 36864 + lw * 3072, vb2, 3);
        sb = next_slot(sb);
      };
      VBAR(0);
      poll(rw, FL_6 + (pm * 16 + kh * 8) * 32 + (lane & 15) * 16, tag);
      i_w4(0);
#pragma unroll 1
      for (int kt = 1; kt < 18; ++kt) { i_w4(kt); VBAR(12); }
      const unsigned fo = FL_5 + pm * 512 + (lane & 31) * 16;
      const u32x4 f = peek(rw, fo);
      glds4(a0 + 18 * 6144, ldsa + sb + lw * 6144, l16);
      const char* b = b0 + (size_t)(18 * 96 + lw * 16) * 2048;
      panel(b + pn * 128, 2048, ldsa + sb + 24576 + lw * 2048, vb2, 2);
      panel(b + (pn ^ 8) * 128, 2048, ldsa + sb + 36864 + lw * 2048, vb2, 2);
      sb = next_slot(sb);
      VBAR(8);
      check(f, rw, fo, tag);
    }
#pragma unroll 1
    for (int kt = 0; kt < 7; ++kt) { i_nf(WS_O, wo, kt); VBAR(12); }
    {
      const unsigned fo = FL_7 + (pm * 16 + (pn ^ 8)) * 32 + (lane & 1) * 16;
      const u32x4 f = peek(rw, fo);
      i_nf(WS_O, wo, 7);
      VBAR(12);
      check(f, rw, fo, tag);
      const char* a = ws + WS_P + (size_t)(pm * 16 + (pn ^ 8)) * 32768 + lw * 8192;
      glds4(a, ldsa + sb + lw * 8192, l16);
      glds4(a + 4096, ldsa + sb + lw * 8192 + 4096, l16);
    }
    VBAR(8);
    VBAR(0);
    if (bid == 0 && lw == 0) {
#pragma unroll 1
      for (int i = 0; i < 8; ++i) poll(rw, FL_1 + i * 1024 + l16, tag);
      if (lane == 0) __builtin_amdgcn_raw_buffer_store_b32(tag, rw, FL_E, 0, 16);
    }
    return;
  }

  const int wm = wid & 3, wn = wid >> 2;
  int hh = lane >> 5;
  int mrow = pm * 128 + wm * 32 + perm23(lane & 31);
  unsigned tag;
  {
    const int row = pm * 128 + pn * 8 + wid;
    const u32x4 a = *(const u32x4*)(x + (size_t)row * 2048 + lane * 16), b = *(const u32x4*)(x + (size_t)row * 2048 + 1024 + lane * 16);
    const u32x4 ga = *(const u32x4*)(gw + lane * 16), gb = *(const u32x4*)(gw + 1024 + lane * 16);
    tag = __builtin_amdgcn_readfirstlane(__builtin_amdgcn_raw_buffer_load_b32(rw, FL_E, 0, 16)) + 1u;
    if (wid < 3) touch(wid == 0 ? w1 + bid * 8192 : x + (bid * 2 + wid - 1) * 8192, ldsa + SPARE + 1024, lane * 128);
    float v[16], w[16];
#pragma unroll
    for (int e = 0; e < 4; ++e) {
      v[2 * e] = bflo(a[e]); v[2 * e + 1] = bfhi(a[e]); v[8 + 2 * e] = bflo(b[e]); v[9 + 2 * e] = bfhi(b[e]);
      w[2 * e] = bflo(ga[e]); w[2 * e + 1] = bfhi(ga[e]); w[8 + 2 * e] = bflo(gb[e]); w[9 + 2 * e] = bfhi(gb[e]);
    }
    float s = 0.f, d = 0.f;
#pragma unroll
    for (int e = 0; e < 16; ++e) { s += v[e]; d += rbf(v[e] * w[e]); }
    s = wave_sum(s); d = wave_sum(d);
    const float mu = s * (1.0f / 1024.0f);
    float q = 0.f;
#pragma unroll
    for (int e = 0; e < 16; ++e) { const float t = v[e] - mu; q += t * t; }
    q = wave_sum(q);
    if (lane < 2) {
      const float rstd = rsqrtf(q * (1.0f / 1024.0f) + 1e-5f);
      const float g = rbf(1.0f / (1.0f + __expf(-rbf(d))));
      u32x4 o; o[0] = asu(lane ? g : mu); o[1] = tag; o[2] = asu(lane ? rbf(1.0f - g) : rstd); o[3] = tag;
      st16(rw, WS_ST1 + row * 32 + lane * 16, o);
    }
  }
  auto nohook = [](int) {};
  int sb = 0;
  float og;
  LBAR();
  STAMP(1);
  {
    f32x16 acc[1] = {};
    Gemm<1, 4, 2, 0, 8, 128, 0, 0> g(lds, lane, wm, 32768, wn * 4);
    g.first(sb);
    g.run(acc, sb, 0, 5, [&](int kt) {
      if (kt + 3 == wid) touch(kt == 3 ? w2 + bid * 8192 : wqkv + (bid * 3 + (kt & 3)) * 8192, ldsa + SPARE + 1024, lane * 128);
    });
    u32x4 s0 = ld16(rw, WS_ST1 + mrow * 32), s1 = ld16(rw, WS_ST1 + mrow * 32 + 16);
    g.run(acc, sb, 5, 7, nohook);
    g.last<4>(acc, sb);
    STAMP(2);
    for (int it = 0; it < SPIN_CAP; ++it) {
      if (__all(s0[1] == tag && s0[3] == tag && s1[3] == tag)) break;
      spin_pause();
      s0 = ld16(rw, WS_ST1 + mrow * 32); s1 = ld16(rw, WS_ST1 + mrow * 32 + 16);
    }
    og = asf(s1[2]);
    const float rs = asf(s0[2]) * GS, nmu = -asf(s0[0]);
    const int grp = pn >> 2, md = mrow + ((grp & 1) ? -(1 + (grp >> 1)) : (1 + (grp >> 1)));
    const bool ok = (unsigned)md < 2048u;
    const int mw = md & 2047;
    const unsigned dst = WS_HS + ((mw >> 5) * 64 + pn * 4 + wn * 2) * 1024 + (perm23(mw & 31) + (lane & 32)) * 16;
#pragma unroll
    for (int s = 0; s < 2; ++s) {
      LAS char* cp = lds + SPARE + wn * 128 + s * 64 + hh * 32;
      const f32x4 c0 = *(LAS f32x4*)cp + *(LAS f32x4*)(cp + 256), c1 = *(LAS f32x4*)(cp + 16) + *(LAS f32x4*)(cp + 272);
      float hv[8];
#pragma unroll
      for (int e = 0; e < 8; ++e) hv[e] = gelu_s(rs * __builtin_fmaf(nmu, e < 4 ? c0[e & 3] : c1[e & 3], acc[0][8 * s + e]));
      u32x4 o;
#pragma unroll
      for (int e = 0; e < 4; ++e) o[e] = ok ? pk2(hv[2 * e], hv[2 * e + 1]) : 0u;
      st16(rw, dst + s * 1024, o);
    }
  }
  STAMP(3);
  {
    f32x16 acc[3] = {};
    Gemm<3, 2, 2, 0, 4, 128, 8192, 4> g(lds, lane, wm, 16384, wn * 4);
    g.first(sb);
    g.run(acc, sb, 0, 15, [&](int kt) {
      if (kt == 1) publish(rw, FL_1 + ((pm * 4 + wm) * 32 + pn * 2 + wn) * 4, tag, lane);
      if (kt == wid + 2) {
        const unsigned u = (bid * 8 + wid) * 8192;
        touch(u < 7 * MB ? w3 + u : u < 14 * MB ? w4 + (u - 7 * MB) : wo + (u - 14 * MB), ldsa + SPARE + 1024, lane * 128);
      }
    });
    g.last<2>(acc, sb);
    STAMP(4);
    const unsigned dst = WS_Q + (((pn * 2 + wn) * 64 + pm * 4 + wm) * 2) * 1024 + lane * 16;
#pragma unroll
    for (int ni = 0; ni < 3; ++ni)
#pragma unroll
      for (int s = 0; s < 2; ++s)
        st16(rw, dst + ni * 4 * MB + s * 1024, pack8(acc[ni], s, ni == 0 ? 0.17677669529663687f * 1.4426950408889634f : 1.0f));
  }
  STAMP(5);
  {
    f32x16 acc[1] = {};
    Gemm<1, 4, 2, 1, 8, 128, 0, 0> g(lds, lane, wm, 32768, wn * 4);
    g.first(sb);
    g.run(acc, sb, 0, P2, nohook);
    publish(rw, FL_2 + ((pn * 2 + wn) * 64 + pm * 4 + wm) * 4, tag, lane);
    u32x4 xv[2];
#pragma unroll
    for (int s = 0; s < 2; ++s) xv[s] = *(const u32x4*)(x + (size_t)mrow * 2048 + pn * 128 + wn * 64 + s * 32 + hh * 16);
    g.run(acc, sb, P2, 7, nohook);
    g.last<4>(acc, sb);
    STAMP(6);
    float v[16];
#pragma unroll
    for (int s = 0; s < 2; ++s) {
      u32x4 o;
#pragma unroll
      for (int e = 0; e < 4; ++e) {
        o[e] = pk2(__builtin_fmaf(acc[0][8 * s + 2 * e], GSI, bflo(xv[s][e])), __builtin_fmaf(acc[0][8 * s + 2 * e + 1], GSI, bfhi(xv[s][e])));
        v[8 * s + 2 * e] = bflo(o[e]); v[8 * s + 2 * e + 1] = bfhi(o[e]);
      }
      *(LAS u32x4*)(lds + SPARE + (wm * 4 + wn * 2 + s) * 1024 + lane * 16) = o;
    }
    float sm = 0.f;
#pragma unroll
    for (int e = 0; e < 16; ++e) sm += v[e];
    const float mean = xhalf_sum(sm) * (1.0f / 32.0f);
    float q = 0.f;
#pragma unroll
    for (int e = 0; e < 16; ++e) { const float t = v[e] - mean; q += t * t; }
    q = xhalf_sum(q);
    if (lane < 32) {
      u32x4 o; o[0] = asu(mean); o[1] = tag; o[2] = asu(q); o[3] = tag;
      st16(rw, WS_ST2 + ((pn * 2 + wn) * 2048 + (pm * 4 + wm) * 32 + lane) * 16, o);
    }
  }
  STAMP(7);
  {
    Att a;
    a.o = (f32x16){};
    a.l = (f32x4){};
    attend<true>(a, lds, sb, ATT1, lane, wid, nohook);
    STAMP(8);
    if (wn == 0) w3_phase<4>(lds, sb, rw, lane, wm, wn, pm, pn, ws);
    else w3_phase<3>(lds, sb, rw, lane, wm, wn, pm, pn, ws);
    STAMP(10);
    asm volatile("" : "+v"(lane), "+v"(a.m));
    hh = lane >> 5;
    mrow = pm * 128 + wm * 32 + perm23(lane & 31);
    a.l[1] = a.l[2] = a.l[3] = 0.f;
    const int mb = qg * 8 + wid;
    const unsigned go = WS_ST1 + (mb * 32 + perm23(lane & 31)) * 32 + 16;
    u32x2 gv = ld8(rw, go);
    attend<false>(a, lds, sb, 8 - ATT1, lane, wid, [&](int st) {
      if (st == P6) publish(rw, FL_6 + ((pm * 16 + pn) * 8 + wid) * 4, tag, lane);
    });
    STAMP(11);
    if (__any(!(a.l[0] <= 1.152921504606847e18f))) attend_exact(a, rw, head, lane);
    for (int it = 0; it < SPIN_CAP; ++it) {
      if (__all(gv[1] == tag)) break;
      spin_pause();
      gv = ld8(rw, go);
    }
    const float c = asf(gv[0]) / a.l[0];
#pragma unroll
    for (int s = 0; s < 2; ++s) st16(rw, WS_O + (mb * 64 + head * 2 + s) * 1024 + lane * 16, pack8(a.o, s, c));
  }
  STAMP(12);
  {
    f32x16 acc[2] = {};
    {
      Gemm<2, 3, 2, 1, 6, 128, 12288, 0> g(lds, lane, wm, 24576, wn * 4);
      g.first(sb);
      g.run(acc, sb, 0, 18, [&](int kt) {
        if (kt == 3) publish(rw, FL_5 + ((qg * 8 + wid) * 32 + head) * 4, tag, lane);
      });
      g.last<1>(acc, sb);
    }
    STAMP(13);
#pragma unroll
    for (int i = 0; i < 4; ++i) {
      u32x4 t;
#pragma unroll
      for (int e = 0; e < 4; ++e) t[e] = asu(acc[1][4 * i + e]);
      st16(rw, WS_P + (pm * 16 + pn) * 32768 + (wid * 4 + i) * 1024 + lane * 16, t);
    }
    const float ogs = og * GSI;
    f32x16 ac[1];
#pragma unroll
    for (int e = 0; e < 16; ++e) ac[0][e] = acc[0][e] * ogs;
    Gemm<1, 4, 2, 1, 8, 128, 0, 0> g(lds, lane, wm, 32768, wn * 4);
    g.first(sb);
    g.run(ac, sb, 0, 7, [&](int kt) {
      if (kt == P7) publish(rw, FL_7 + ((pm * 16 + pn) * 8 + wid) * 4, tag, lane);
    });
    g.last<4>(ac, sb);
    STAMP(14);
#pragma unroll
    for (int s = 0; s < 2; ++s) {
      const u32x4 xv = *(LAS u32x4*)(lds + SPARE + (wm * 4 + wn * 2 + s) * 1024 + lane * 16);
      const f32x4 pa = *(LAS f32x4*)(lds + sb + (wid * 4 + s * 2) * 1024 + lane * 16), pb = *(LAS f32x4*)(lds + sb + (wid * 4 + s * 2 + 1) * 1024 + lane * 16);
      u32x4 o;
#pragma unroll
      for (int e = 0; e < 4; ++e) {
        const float p0 = e < 2 ? pa[2 * e] : pb[2 * e - 4], p1 = e < 2 ? pa[2 * e + 1] : pb[2 * e - 3];
        o[e] = pk2(__builtin_fmaf(og, bflo(xv[e]), __builtin_fmaf(ogs, p0, ac[0][8 * s + 2 * e])),
                   __builtin_fmaf(og, bfhi(xv[e]), __builtin_fmaf(ogs, p1, ac[0][8 * s + 2 * e + 1])));
      }
      *(u32x4*)(out + (size_t)mrow * 2048 + pn * 128 + wn * 64 + s * 32 + hh * 16) = o;
    }
  }
  STAMP(15);
}

extern "C" void kernel_launch(void* const* d_in, const int* in_sizes, int n_in, void* d_out, int out_size, void* d_ws,
                              size_t ws_size, hipStream_t stream) {
  (void)hipFuncSetAttribute((const void*)k_mega, hipFuncAttributeMaxDynamicSharedMemorySize, LDS_BYTES);
  k_mega<<<256, 768, LDS_BYTES, stream>>>((const char*)d_in[0], (const char*)d_in[1], (const char*)d_in[2], (const char*)d_in[4],
                                          (const char*)d_in[5], (const char*)d_in[6], (const char*)d_in[7], (const char*)d_in[8],
                                          (char*)d_ws, (char*)d_out);
}
```

```cpp
#include <hip/hip_runtime.h>
#include <stdint.h>

typedef __attribute__((ext_vector_type(8))) __bf16 bf16x8;
typedef __attribute__((ext_vector_type(2))) __bf16 bf16x2;
typedef __attribute__((ext_vector_type(4))) short s16x4;
typedef __attribute__((ext_vector_type(16))) float f32x16;
typedef __attribute__((ext_vector_type(4))) float f32x4;
typedef __attribute__((ext_vector_type(4))) unsigned u32x4;
typedef __attribute__((ext_vector_type(2))) unsigned u32x2;
typedef __amdgpu_buffer_rsrc_t rsrc_t;
#define LAS __attribute__((address_space(3)))
#define DEV __device__ __forceinline__

#define WAIT_V(n) asm volatile("s_waitcnt vmcnt(" #n ")" ::: "memory")
#define LBAR() asm volatile("s_waitcnt lgkmcnt(0)\n\ts_barrier" ::: "memory")
#define SCHED() __builtin_amdgcn_sched_barrier(0)
#define MFMA32(a, b, c) __builtin_amdgcn_mfma_f32_32x32x16_bf16(a, b, c, 0, 0, 0)
#define MFMA16(a, b, c) __builtin_amdgcn_mfma_f32_16x16x32_bf16(a, b, c, 0, 0, 0)

constexpr unsigned MB = 1024u * 1024u;
constexpr unsigned WS_Q = 0, WS_K = 4 * MB, WS_V = 8 * MB;
constexpr unsigned WS_HS = 12 * MB, WS_XN = 16 * MB, WS_O = 20 * MB, WS_H = 24 * MB, WS_P = 38 * MB;
constexpr unsigned WS_ST1 = 46 * MB, WS_GT = WS_ST1 + 65536, WS_ST2 = 47 * MB, WS_FL = 48 * MB, WS_END = 49 * MB;
constexpr unsigned FL_E = WS_FL, FL_4 = WS_FL + 4096, FL_5 = FL_4 + 8192, FL_6 = FL_5 + 8192, FL_7 = FL_6 + 8192,
                   FL_1 = WS_FL + 131072, FL_2 = WS_FL + 262144;
constexpr int SLOT = 49152, SPARE = 3 * SLOT, LDS_BYTES = 163840, TAGW = SPARE + 1536;
constexpr int ATT1 = 4;
constexpr float GS = 0.46866869f, GA = 4.9122295f, GSI = 1.0f / GS;

#define STAMP(i)

DEV unsigned pk2(float a, float b) {
  bf16x2 v; v[0] = (__bf16)a; v[1] = (__bf16)b;
  return __builtin_bit_cast(unsigned, v);
}
DEV unsigned pk2s(float a, float b) { asm("" : "+v"(a)); asm("" : "+v"(b)); return pk2(a, b); }
DEV float bflo(unsigned u) { return __builtin_bit_cast(float, u << 16); }
DEV float bfhi(unsigned u) { return __builtin_bit_cast(float, u & 0xffff0000u); }
DEV float rbf(float a) { return (float)(__bf16)a; }
DEV float asf(unsigned u) { return __builtin_bit_cast(float, u); }
DEV unsigned asu(float f) { return __builtin_bit_cast(unsigned, f); }
DEV float gelu_s(float v) {
  const float e = __builtin_amdgcn_exp2f(-v * __builtin_fmaf(v, v, GA));
  return v * __builtin_amdgcn_rcpf(1.0f + e);
}
typedef __attribute__((ext_vector_type(2))) float f32x2;
DEV unsigned gelu_p(float a, float b) {
  const f32x2 v = {a, b}, ga = {-GA, -GA}, one = {1.0f, 1.0f};
  const f32x2 t = v * __builtin_elementwise_fma(-v, v, ga);
  f32x2 e = {__builtin_amdgcn_exp2f(t[0]), __builtin_amdgcn_exp2f(t[1])};
  e += one;
  const f32x2 r = {__builtin_amdgcn_rcpf(e[0]), __builtin_amdgcn_rcpf(e[1])}, h = v * r;
  return pk2(h[0], h[1]);
}
DEV float xhalf_sum(float v) {
  const auto r = __builtin_amdgcn_permlane32_swap(asu(v), asu(v), false, false);
  return asf(r[0]) + asf(r[1]);
}
DEV float xhalf_max(float v) {
  const auto r = __builtin_amdgcn_permlane32_swap(asu(v), asu(v), false, false);
  return fmaxf(asf(r[0]), asf(r[1]));
}
DEV void st16(rsrc_t r, unsigned off, u32x4 v) { __builtin_amdgcn_raw_buffer_store_b128(v, r, off, 0, 16); }
#define QAUX 16
DEV void st16q(rsrc_t r, unsigned off, u32x4 v) { __builtin_amdgcn_raw_buffer_store_b128(v, r, off, 0, QAUX); }
DEV u32x4 ld16(rsrc_t r, unsigned off) { return __builtin_amdgcn_raw_buffer_load_b128(r, off, 0, 16); }
DEV u32x2 ld8(rsrc_t r, unsigned off) { return __builtin_amdgcn_raw_buffer_load_b64(r, off, 0, 16); }
#define INV 0
#define WPOL ""
#define IPOL ""
#define SCL " sc1" IPOL
#define CSD 4
#define PQ 1
#define LPK 1
#define OUTT 1
#define PV 2
#define TVS 10
#define QB 0
#define P6 2
#define P5 3
#define P1 1
#define D3 8
#define D3B 6
#define PS3B 2
#define PS3 2
#define DN 10
#define PSN 4
#define DQ 14
#define PSQ 4
#define D4 14
#define PS4 6
#define PG 0
#define PA 0
#define PB_ 0
#define PRIO(n) __builtin_amdgcn_s_setprio(n)
#define E1 10
#define XPN 0
#define PF 31
#define KO 2
#define PFD 4
#define CPF 0
#define GP0 0
#define GP1 8
#define OST " sc1"
#define SELZ 11
#define PZ 2
#define US 0
#define PX 0
#define PY 1
#define PW 0
#define PFK 1
#define T0 2
#define TS_ 2
#define SPIN 20
constexpr int SPIN_CAP = 1 << SPIN;
DEV void spin_pause() { __builtin_amdgcn_s_sleep(4); asm volatile("" ::: "memory"); }
DEV void publish(rsrc_t r, unsigned off, unsigned tag, int lane, int n = 1) {
  WAIT_V(0);
  if (lane < n) __builtin_amdgcn_raw_buffer_store_b32(tag, r, off + lane * 4, 0, 16);
}
DEV void poll(rsrc_t r, unsigned off, unsigned tag) {
#pragma unroll 1
  for (int it = 0; it < SPIN_CAP; ++it) {
    const u32x4 v = ld16(r, off);
    if (__all(v[0] == tag && v[1] == tag && v[2] == tag && v[3] == tag)) break;
    spin_pause();
  }
}

DEV u32x4 peek(rsrc_t r, unsigned off) {
  u32x4 v;
  asm volatile("buffer_load_dwordx4 %0, %1, %2, 0 offen sc1" : "=v"(v) : "v"(off), "s"(r) : "memory");
  return v;
}
DEV void check(u32x4 v, rsrc_t r, unsigned off, unsigned tag) {
  asm volatile("" : "+v"(v)::"memory");
  if (!__all(v[0] == tag && v[1] == tag && v[2] == tag && v[3] == tag)) poll(r, off, tag);
}

#define GLDS_ "global_load_lds_dwordx4 "
DEV void glds1(const char* sb, unsigned l, unsigned v) {
  asm volatile("s_mov_b32 m0, %1\n\ts_nop 0\n\t" GLDS_ "%2, %0" WPOL ::"s"(sb), "s"(l), "v"(v) : "memory");
}
DEV void glds2(const char* sb, unsigned l, unsigned v) {
  asm volatile("s_mov_b32 m0, %1\n\ts_nop 0\n\t" GLDS_ "%2, %0" SCL "\n\t" GLDS_ "%2, %0 offset:1024" SCL ::"s"(sb), "s"(l), "v"(v) : "memory");
}
DEV void glds4(const char* sb, unsigned l, unsigned v) {
  asm volatile("s_mov_b32 m0, %1\n\ts_nop 0\n\t" GLDS_ "%2, %0" SCL "\n\t" GLDS_ "%2, %0 offset:1024" SCL "\n\t" GLDS_ "%2, %0 offset:2048" SCL "\n\t" GLDS_ "%2, %0 offset:3072" SCL
               ::"s"(sb), "s"(l), "v"(v) : "memory");
}
DEV void touch(const char* sb, unsigned l, unsigned v) {
  const uint64_t a = (uint64_t)sb;
  const unsigned hi = __builtin_amdgcn_readfirstlane((unsigned)(a >> 32)), lo = __builtin_amdgcn_readfirstlane((unsigned)a);
  sb = (const char*)(((uint64_t)hi << 32) | (uint64_t)lo);
  asm volatile("s_mov_b32 m0, %1\n\ts_nop 4\n\tglobal_load_lds_dword %2, %0" ::"s"(sb), "s"(l), "v"(v) : "memory");
}
DEV void touch64(const char* p, unsigned l) {
  asm volatile("s_mov_b32 m0, %1\n\ts_nop 0\n\tglobal_load_lds_dword %0, off" ::"v"(p), "s"(l) : "memory");
}
DEV unsigned touch_v(const char* sb, unsigned v) {
  unsigned r;
  asm volatile("global_load_dword %0, %1, %2" : "=v"(r) : "v"(v), "s"(sb) : "memory");
  return r;
}
DEV unsigned slow_line(const char* w) { return ((3u - ((unsigned)(uintptr_t)w >> 7)) & 7u) * 128u; }
template <int N> DEV void vwait() { asm volatile("s_waitcnt vmcnt(%0) lgkmcnt(0)" ::"n"(N) : "memory"); }
template <int N> DEV void vbar_() { asm volatile("s_waitcnt vmcnt(%0) lgkmcnt(0)\n\ts_barrier" ::"n"(N) : "memory"); }
#define VBAR(N) vbar_<N>()
DEV int next_slot(int sb) { return sb == 2 * SLOT ? 0 : sb + SLOT; }
DEV bf16x8 cat(u32x2 a, u32x2 b) {
  u32x4 c; c[0] = a[0]; c[1] = a[1]; c[2] = b[0]; c[3] = b[1];
  return __builtin_bit_cast(bf16x8, c);
}
DEV u32x2 rd_tr(LAS char* p) { return __builtin_bit_cast(u32x2, __builtin_amdgcn_ds_read_tr16_b64_v4i16((LAS s16x4*)p)); }
template <int AMODE, int RB> DEV int wfrag_off(int lane, int ct) {
  const int g = lane >> 4, q = (lane >> 2) & 3, p = lane & 3;
  const int r = AMODE == 0 ? 8 * g + q : 4 * g + q;
  const int f = RB != 128 ? (r >> 2) & 1 : AMODE == 0 ? (q >> 1) | ((g & 1) << 1) : (r >> 1) & 3;
  return r * RB + ((ct ^ f) << 5) + p * 8;
}
#define MAC16(c, a, b) asm("v_mfma_f32_16x16x32_bf16 %0, %1, %2, %0" : "+v"(c) : "v"(a), "v"(b))

template <int TM, int TN, int TS, int KS, int DIST, int PAUSE_, int AMODE, int RB, int NW, int NSTR, int VMASK>
struct Gemm {
  static constexpr int M = TM * (TN - 2) + 2 * TS, RK = TM + 2 * TN, MS = KS * M, R = KS * RK, PB = MS - DIST;
  static constexpr int NA = AMODE == 0 || TS < TM ? 2 : 1;
  LAS char* pa[NA];
  LAS char* pw[NW];
  bf16x8 xf[KS][TM];
  u32x2 wl[KS][TN], wh[KS][TN];
  DEV Gemm(LAS char* l, int lane, int mt, int boff, int c0, int c2 = 2) {
    static_assert(legal(), "read schedule");
    const int row = mt * 32 + (lane & 15);
#pragma unroll
    for (int i = 0; i < NA; ++i)
      pa[i] = l + (AMODE == 0 ? (row * 128 + (((lane >> 4) ^ ((row >> 1) & 7)) << 4)) ^ (i << 6) : (mt ^ i) * KS * 2048 + lane * 16);
#pragma unroll
    for (int i = 0; i < NW; ++i) pw[i] = l + boff + wfrag_off<AMODE, RB>(lane, (i < 2 ? c0 : c2) + (i & 1));
  }
  static constexpr int start(int ni) { return ni < TN - 2 ? ni * TM : (TN - 2) * TM + (ni - TN + 2) * TS; }
  static constexpr int gap(int j) { return PB + j * (MS - PAUSE_) / R; }
  static constexpr bool legal() {
    for (int j = 0; j < R; ++j) {
      const int s = j / RK, i = j % RK;
      const bool isx = i >= 2 && i < 2 + TM;
      const int ni = (i < 2 ? i : i - TM) >> 1;
      if (gap(j) >= MS + s * M + (isx ? i - 2 : start(ni))) return false;
      if (gap(j) < s * M + (isx ? M : start(ni + 1)) - 1) return false;
      if (gap(j) >= MS + PB - (KS - 1 - s) * M) return false;
    }
    return true;
  }
  DEV void rd(int j) {
    const int s = j / RK, i = j % RK;
    if (i >= 2 && i < 2 + TM) {
      const int mi = i - 2;
      if (AMODE == 0) xf[s][mi] = *(LAS bf16x8*)(pa[s & 1] + (s >> 1) * 16384 + mi * 2048);
      else if (NA == 2) xf[s][mi] = *(LAS bf16x8*)(pa[mi >> 1] + s * 2048 + (mi & 1) * 1024);
      else xf[s][mi] = *(LAS bf16x8*)(pa[0] + s * 2048 + (mi >> 1) * KS * 2048 + (mi & 1) * 1024);
    } else {
      const int t = i < 2 ? i : i - TM, ni = t >> 1;
      LAS char* p = pw[ni % NW] + (ni / NW) * NSTR + s * 32 * RB;
      if (t & 1) wh[s][ni] = rd_tr(p + (AMODE == 0 ? 4 : 16) * RB); else wl[s][ni] = rd_tr(p);
    }
  }
  DEV void first() {
#pragma unroll
    for (int j = 0; j < R; ++j) if (gap(j) < MS) { rd(j); SCHED(); }
  }
  template <bool LAST, int KL>
  DEV void step(f32x4 (&acc)[TM][TN], int& sb) {
    int d = 0;
#pragma unroll
    for (int i = 0; i < KL * M; ++i) {
      const int s = i / M, r = i % M, ni = r < start(TN - 2) ? r / TM : TN - 2 + (r - start(TN - 2)) / TS, mi = r - start(ni);
      const bf16x8 w = cat(wl[s][ni], wh[s][ni]);
      if ((VMASK >> ni) & 1) MAC16(acc[mi][ni], xf[s][mi], w);
      else MAC16(acc[mi][ni], w, xf[s][mi]);
      SCHED();
      if (i == PB - (KS - KL) * M) { LBAR(); SCHED(); }
#pragma unroll
      for (int j = 0; j < R; ++j) {
        if (gap(j) == i + MS && j < KL * RK) rd(j);
        if (gap(j) == i && !LAST) rd(j);
      }
      if (!LAST) {
        if (i == PB - 3 - (NA + NW - 1) / 2) { asm volatile("" : "+s"(sb)); d = sb == 2 * SLOT ? -2 * SLOT : SLOT; asm volatile("" : "+s"(d)); }
        if (i == PB - 2 - (NA + NW - 1) / 2) { sb += d; asm volatile("" : "+s"(sb)); }
#pragma unroll
        for (int j = 0; j < NA + NW; ++j)
          if (i == PB - 1 - j / 2) {
            if (j < NW) { pw[NW - 1 - j] += d; asm volatile("" : "+v"(pw[NW - 1 - j])); }
            else { pa[j - NW] += d; asm volatile("" : "+v"(pa[j - NW])); }
          }
      }
      SCHED();
    }
    static_assert(gap(R - 1) - MS <= PB - 1 && gap(R - 5) - MS <= PB - 2 && gap(R - 2 * TN + 1) - MS <= PB - 1 - (NA + NW - 1) / 2, "address update");
  }
  template <class H>
  DEV void run(f32x4 (&acc)[TM][TN], int& sb, int k0, int k1, H hook) {
#pragma unroll
    for (int i = 0; i < TM * TN; ++i) if (i % TN < TN - 2 || i / TN < TS) asm volatile("" : "+v"(acc[i / TN][i % TN]));
    asm volatile("s_nop 1");
    SCHED();
#pragma unroll 1
    for (int kt = k0; kt < k1; ++kt) {
      step<false, KS>(acc, sb);
      hook(kt);
    }
  }
  template <int KL = KS>
  DEV void last(f32x4 (&acc)[TM][TN], int& sb) {
    step<true, KL>(acc, sb);
    asm volatile("s_nop 7\n\ts_nop 3");
    SCHED();
    sb = next_slot(sb);
  }
};
DEV void bars(int n) {
#pragma unroll 1
  for (int i = 0; i < n; ++i) asm volatile("s_barrier" ::: "memory");
}

DEV u32x4 pack8(const f32x16& a, int s, float sc) {
  u32x4 r;
#pragma unroll
  for (int e = 0; e < 4; ++e) r[e] = pk2s(a[8 * s + 2 * e] * sc, a[8 * s + 2 * e + 1] * sc);
  return r;
}
DEV u32x4 pack44(const f32x4& a, const f32x4& b, float sc) {
  u32x4 r;
  r[0] = pk2s(a[0] * sc, a[1] * sc); r[1] = pk2s(a[2] * sc, a[3] * sc); r[2] = pk2s(b[0] * sc, b[1] * sc); r[3] = pk2s(b[2] * sc, b[3] * sc);
  return r;
}
DEV u32x4 xrow(u32x4 v) {
  const auto a = __builtin_amdgcn_permlane16_swap(v[0], v[2], false, false), b = __builtin_amdgcn_permlane16_swap(v[1], v[3], false, false);
  u32x4 r; r[0] = a[0]; r[1] = b[0]; r[2] = a[1]; r[3] = b[1];
  return r;
}
DEV float xrow_sum(float v) {
  const auto r = __builtin_amdgcn_permlane16_swap(asu(v), asu(v), false, false);
  return asf(r[0]) + asf(r[1]);
}
DEV float two_sum(float a, float b) {
  const auto r = __builtin_amdgcn_permlane32_swap(asu(a), asu(b), false, false);
  return xrow_sum(asf(r[0]) + asf(r[1]));
}
template <int C> DEV float dpp_add(float v) { return v + asf(__builtin_amdgcn_update_dpp(0, asu(v), C, 0xf, 0xf, true)); }
DEV float half_sum(float v) {
  v = dpp_add<0x140>(dpp_add<0x141>(dpp_add<0x4E>(dpp_add<0xB1>(v))));
  return xrow_sum(v);
}

struct Att {
  bf16x8 q0, q1, sel;
  f32x16 o;
  f32x4 l;
  float m;
};
template <bool FIRST, class H>
DEV void attend(Att& a, LAS char* lds, int& sb, int nsteps, int lane, int wid, H hook) {
  const int lo = lane * 16;
  bf16x8 k0, k1, v0[2], v1[2];
  f32x16 s[2], negm;
  u32x4 p0, p1 = {0u, 0u, 0u, 0u};
  {
    const unsigned one2 = (((lane >> 2) & 1) == ((lane >> 4) & 1) && (lane & SELZ) == 0) ? 0x3f803f80u : 0u;
    const u32x4 t = {one2, one2, one2, one2};
    a.sel = __builtin_bit_cast(bf16x8, t);
  }
  {
    LAS char* p = lds + sb + lo;
    k0 = *(LAS bf16x8*)p; k1 = *(LAS bf16x8*)(p + 1024);
    if (FIRST) { a.q0 = *(LAS bf16x8*)(p + 32768 + wid * 2048); a.q1 = *(LAS bf16x8*)(p + 32768 + wid * 2048 + 1024); }
    v0[0] = *(LAS bf16x8*)(p + 16384); v1[0] = *(LAS bf16x8*)(p + 16384 + 1024);
    v1[1] = __builtin_bit_cast(bf16x8, p1);
    if (FIRST) {
      f32x16 t = {};
      t = MFMA32(k0, a.q0, t);
      t = MFMA32(k1, a.q1, t);
      float m = t[0];
#pragma unroll
      for (int e = 1; e < 16; ++e) m = fmaxf(m, t[e]);
      a.m = xhalf_max(m);
    }
#pragma unroll
    for (int e = 0; e < 16; ++e) negm[e] = -a.m;
    s[0] = MFMA32(k0, a.q0, negm);
    s[0] = MFMA32(k1, a.q1, s[0]);
    SCHED();
    k0 = *(LAS bf16x8*)(p + 2048); k1 = *(LAS bf16x8*)(p + 3072);
    SCHED();
  }
#define EX(i) e[i] = __builtin_amdgcn_exp2f(s[c][i])
#pragma unroll 1
  for (int st = 0; st < nsteps; ++st) {
    const int sbn = next_slot(sb);
    LAS char* pc = lds + sb + lo;
    LAS char* pn = lds + sbn + lo;
#pragma unroll
    for (int t = 0; t < 8; ++t) {
      const int c = t & 1;
      float e[16];
      a.o = MFMA32(v1[c ^ 1], __builtin_bit_cast(bf16x8, p1), a.o);
      SCHED();
      {
        LAS char* p = (t < 7 ? pc + (t + 1) * 2048 : pn) + 16384;
        v0[c ^ 1] = *(LAS bf16x8*)p; v1[c ^ 1] = *(LAS bf16x8*)(p + 1024);
      }
      EX(0); EX(1); EX(2); EX(3);
      SCHED();
      a.l = MFMA16(a.sel, __builtin_bit_cast(bf16x8, p1), a.l);
      SCHED();
      p0[0] = pk2(e[0], e[1]); p0[1] = pk2(e[2], e[3]); EX(4); EX(5);
      SCHED();
      s[c ^ 1] = MFMA32(k0, a.q0, negm);
      SCHED();
      EX(6); EX(7); p0[2] = pk2(e[4], e[5]); p0[3] = pk2(e[6], e[7]);
      SCHED();
      s[c ^ 1] = MFMA32(k1, a.q1, s[c ^ 1]);
      SCHED();
      if (t == 6) { LBAR(); SCHED(); }
      {
        LAS char* p = t < 6 ? pc + (t + 2) * 2048 : pn + (t - 6) * 2048;
        k0 = *(LAS bf16x8*)p; k1 = *(LAS bf16x8*)(p + 1024);
      }
      EX(8); EX(9); EX(10); EX(11);
      SCHED();
      a.o = MFMA32(v0[c], __builtin_bit_cast(bf16x8, p0), a.o);
      SCHED();
      p1[0] = pk2(e[8], e[9]); p1[1] = pk2(e[10], e[11]); EX(12); EX(13);
      SCHED();
      a.l = MFMA16(a.sel, __builtin_bit_cast(bf16x8, p0), a.l);
      SCHED();
      EX(14); EX(15); p1[2] = pk2(e[12], e[13]); p1[3] = pk2(e[14], e[15]);
      SCHED();
      if (t == 2) hook(st);
    }
    sb = sbn;
  }
#undef EX
  a.o = MFMA32(v1[1], __builtin_bit_cast(bf16x8, p1), a.o);
  a.l = MFMA16(a.sel, __builtin_bit_cast(bf16x8, p1), a.l);
}
DEV void attend_exact(Att& a, rsrc_t rw, int head, int lane) {
  const unsigned kb = WS_K + head * 131072 + lane * 16;
  float m = -INFINITY;
#pragma unroll 1
  for (int t = 0; t < 64; ++t) {
    const bf16x8 k0 = __builtin_bit_cast(bf16x8, ld16(rw, kb + t * 2048)), k1 = __builtin_bit_cast(bf16x8, ld16(rw, kb + t * 2048 + 1024));
    f32x16 s = {};
    s = MFMA32(k0, a.q0, s);
    s = MFMA32(k1, a.q1, s);
#pragma unroll
    for (int e = 0; e < 16; ++e) m = fmaxf(m, s[e]);
  }
  m = xhalf_max(m);
  a.o = (f32x16){};
  a.l = (f32x4){};
#pragma unroll 1
  for (int t = 0; t < 64; ++t) {
    const bf16x8 k0 = __builtin_bit_cast(bf16x8, ld16(rw, kb + t * 2048)), k1 = __builtin_bit_cast(bf16x8, ld16(rw, kb + t * 2048 + 1024));
    const bf16x8 v0 = __builtin_bit_cast(bf16x8, ld16(rw, kb + 4 * MB + t * 2048)), v1 = __builtin_bit_cast(bf16x8, ld16(rw, kb + 4 * MB + t * 2048 + 1024));
    f32x16 s = {};
    s = MFMA32(k0, a.q0, s);
    s = MFMA32(k1, a.q1, s);
    u32x4 p0, p1;
#pragma unroll
    for (int e = 0; e < 4; ++e) {
      p0[e] = pk2(__builtin_amdgcn_exp2f(s[2 * e] - m), __builtin_amdgcn_exp2f(s[2 * e + 1] - m));
      p1[e] = pk2(__builtin_amdgcn_exp2f(s[8 + 2 * e] - m), __builtin_amdgcn_exp2f(s[9 + 2 * e] - m));
    }
    a.o = MFMA32(v0, __builtin_bit_cast(bf16x8, p0), a.o);
    a.l = MFMA16(a.sel, __builtin_bit_cast(bf16x8, p0), a.l);
    a.o = MFMA32(v1, __builtin_bit_cast(bf16x8, p1), a.o);
    a.l = MFMA16(a.sel, __builtin_bit_cast(bf16x8, p1), a.l);
  }
}

template <int TS>
DEV void w3_phase(LAS char* lds, int& sb, rsrc_t rw, int lane, int bm, int bn, int pm, int pn, char* ws) {
  f32x4 acc[4][4] = {};
  const int mt = TS == 4 ? bm : bm + bn, c0 = TS == 4 ? bn * 4 : 8 + bn * 2;
  Gemm<4, 4, TS, 2, TS == 4 ? D3 : D3B, TS == 4 ? PS3 : PS3B, 1, 448, TS == 4 ? 2 : 4, 64, 0> g(lds + sb, lane, mt, 16384, c0, 12);
  g.first();
  g.run(acc, sb, 0, 15, [](int) {});
  g.last(acc, sb);
  STAMP(9);
  const unsigned dst = WS_H + (pm * 4 * 224 + pn * 14) * 1024 + lane * 16;
#pragma unroll
  for (int j = 0; j < 2; ++j)
#pragma unroll
    for (int mi = 0; mi < (j ? TS : 4); ++mi) {
      u32x4 o;
#pragma unroll
      for (int e = 0; e < 4; ++e) {
        const float u0 = acc[mi][2 * j + (e >> 1)][2 * (e & 1)], u1 = acc[mi][2 * j + (e >> 1)][2 * (e & 1) + 1];
        o[e] = j * 4 + mi >= GP0 && j * 4 + mi < GP1 ? gelu_p(u0, u1) : pk2(gelu_s(u0), gelu_s(u1));
      }
      st16(rw, dst + ((mt ^ (mi >> 1)) * 224 + (j == 0 ? c0 : TS == 4 ? c0 + 2 : 12) + (mi & 1)) * 1024, o);
    }
}

__global__ __launch_bounds__(768) void k_mega(const char* __restrict__ x, const char* __restrict__ wqkv, const char* __restrict__ wo,
                                              const char* __restrict__ w1, const char* __restrict__ w2, const char* __restrict__ w3,
                                              const char* __restrict__ w4, const char* __restrict__ gw, char* __restrict__ ws,
                                              char* __restrict__ out) {
  extern __shared__ __attribute__((aligned(16))) char smem[];
  LAS char* lds = (LAS char*)smem;
  const unsigned ldsa = (unsigned)(uintptr_t)lds;
  const int bid = blockIdx.x, xcd = bid & 7, idx = bid >> 3;
  const int pm = (xcd >> 1) * 4 + (idx & 3), pn = (xcd & 1) * 8 + ((idx >> 2) ^ XPN);
  const int head = xcd * 4 + (idx & 3), qg = idx >> 2;
  int lane = threadIdx.x & 63;
  const int wid = __builtin_amdgcn_readfirstlane(threadIdx.x >> 6);
  const rsrc_t rw = __builtin_amdgcn_make_buffer_rsrc(ws, 0, WS_END, 0x00020000);
  STAMP(0);

  if (wid >= 8) {
    const int lw = wid - 8;
    __builtin_amdgcn_s_setprio(3);
    const unsigned l16 = lane * 16;
    const unsigned vx0 = (lane >> 3) * 2048 + (((lane & 7) ^ (lane >> 4)) << 4), vx1 = vx0 ^ 64;
    const unsigned vs0 = ((lane & 7) ^ (((lane >> 4) & 1) << 1)) << 4;
    const unsigned vb1 = (lane >> 3) * 2048 + vs0, vb6 = (lane >> 3) * 6144 + vs0;
    const unsigned vb2 = (lane >> 3) * 2048 + (((lane & 7) ^ ((lane >> 4) << 1)) << 4);
    int sb = 0;
    auto panel = [&](const char* src, unsigned bstr, unsigned l, unsigned v, unsigned vo, int n) {
#pragma unroll
      for (int i = 0; i < 4; ++i) if (i < n) glds1(src + (size_t)i * 8 * bstr, l + i * 1024, (i & 1) ? vo : v);
    };
    auto nb = [&](const char* w, int kt, unsigned v, unsigned vo) {
      panel(w + (size_t)(kt * 128 + lw * 32) * 2048 + pn * 128, 2048, ldsa + sb + 32768 + lw * 4096, v, vo, 4);
    };
    auto i_v = [&](int kt) {
      const char* a = x + (size_t)(pm * 128 + (lw & 1) * 64) * 2048 + kt * 256 + (lw >> 1) * 128;
#pragma unroll
      for (int i = 0; i < 8; ++i) glds1(a + i * 16384, ldsa + sb + lw * 8192 + i * 1024, (i & 1) ? vx1 : vx0);
      panel(wqkv + (size_t)(kt * 128 + lw * 32) * 6144 + 4096 + (pn ^ 3) * 128, 6144, ldsa + sb + 32768 + lw * 4096, vb6, vb6 ^ 64, 4);
      sb = next_slot(sb);
    };
    auto i_nf = [&](unsigned src, const char* w, int kt) {
      const char* a = ws + src + (size_t)((pm * 4 + lw) * 64 + kt * 8) * 1024;
      glds4(a, ldsa + sb + lw * 8192, l16);
      glds4(a + 4096, ldsa + sb + lw * 8192 + 4096, l16);
      nb(w, kt, vb2, vb2);
      sb = next_slot(sb);
    };
    auto i_qkv = [&](int kt) {
      const char* a = x + (size_t)(pm * 128 + lw * 32) * 2048 + kt * 128;
#pragma unroll
      for (int i = 0; i < 4; ++i) glds1(a + i * 16384, ldsa + sb + lw * 4096 + i * 1024, (i & 1) ? vx1 : vx0);
      panel(w1 + (size_t)(kt * 64 + lw * 16) * 2048 + pn * 128, 2048, ldsa + sb + 16384 + lw * 2048, vb1, vb1 ^ 64, 2);
      const char* b = wqkv + (size_t)(kt * 64 + lw * 16) * 6144;
#pragma unroll
      for (int sg = 1; sg < 3; ++sg) panel(b + (sg - 1) * 2048 + (pn ^ sg) * 128, 6144, ldsa + sb + 16384 + sg * 8192 + lw * 2048, vb6, vb6 ^ 64, 2);
      sb = next_slot(sb);
    };
    auto i_att = [&](int st) {
      const char* a = ws + WS_K + (size_t)(lw >> 1) * 4 * MB + head * 131072 + st * 16384 + (lw & 1) * 8192;
      glds4(a, ldsa + sb + lw * 8192, l16);
      glds4(a + 4096, ldsa + sb + lw * 8192 + 4096, l16);
      if (st == 0) glds4(ws + WS_Q + head * 131072 + qg * 16384 + lw * 4096, ldsa + sb + 32768 + lw * 4096, l16);
      sb = next_slot(sb);
    };
    i_qkv(0);
#pragma unroll 1
    for (int kt = 1; kt < 16; ++kt) { i_qkv(kt); VBAR(10); }
    const unsigned tag = __builtin_amdgcn_readfirstlane(*(LAS unsigned*)(lds + TAGW));
#pragma unroll 1
    for (int kt = 0; kt < 7; ++kt) { i_v(kt); VBAR(12); }
    {
      const unsigned fo = FL_1 + ((pm * 2 - 1 + ((lw + 1) >> 1) + ((lane >> 3) & 1)) & 31) * 2048 + pn * 128 + (lane & 7) * 16;
      i_v(7);
      const u32x4 f = peek(rw, fo);
      VBAR(13);
      nb(w2, 0, vb2, vb2);
      vwait<4>();
      check(f, rw, fo, tag);
      const char* a = ws + WS_HS + (size_t)((pm * 4 + lw) * 64) * 1024;
      glds4(a, ldsa + sb + lw * 8192, l16);
      glds4(a + 4096, ldsa + sb + lw * 8192 + 4096, l16);
      sb = next_slot(sb);
    }
#pragma unroll 1
    for (int kt = 1; kt < 7; ++kt) { VBAR(12); i_nf(WS_HS, w2, kt); }
    {
      const unsigned fo = FL_2 + (head * 8 + qg) * 512 + (lane >= 16 ? 384 : lw < 2 ? (lane & 7) * 16 : 128 + lane * 16);
      VBAR(12);
      const u32x4 f = peek(rw, fo);
      i_nf(WS_HS, w2, 7);
      VBAR(12);
      check(f, rw, fo, tag);
    }
    i_att(0);
    VBAR(12); i_att(1);
    VBAR(8);
    const unsigned tu = (bid * 4 + lw) * 8192, tl = ldsa + sb + SLOT - 1024;
    i_att(2);
    touch(tu < 7 * MB ? w3 + tu : wo + (tu - 7 * MB), tl, lane * 128);
    VBAR(9); i_att(3);
    {
      const unsigned so = WS_ST2 + ((lane >> 5) * 8 * 2048 + (pm * 4 + lw) * 32 + (lane & 31)) * 16;
      u32x4 st[8];
      bool late = false;
#pragma unroll 1
      for (int it = 0; it < SPIN_CAP; ++it) {
        bool ok = true;
#pragma unroll
        for (int i = 0; i < 8; ++i) st[i] = ld16(rw, so + i * 32768);
#pragma unroll
        for (int i = 0; i < 8; ++i) ok = ok && st[i][1] == tag && st[i][3] == tag;
        if (__all(ok)) break;
        if (!late) { VBAR(0); late = true; }
        else spin_pause();
      }
      float sm = 0.f, q = 0.f;
#pragma unroll
      for (int i = 0; i < 8; ++i) { sm += asf(st[i][0]); q += asf(st[i][2]); asm("" : "+v"(sm)); }
      const float mu = xhalf_sum(sm) * (1.0f / 16.0f);
      float dd = 0.f;
#pragma unroll
      for (int i = 0; i < 8; ++i) { const float d = asf(st[i][0]) - mu; dd += d * d; }
      q = xhalf_sum(q + 64.0f * dd);
      const float rs1 = __builtin_amdgcn_rsqf(q * (1.0f / 1024.0f) + 1e-5f) * GS, nm1 = -mu * rs1;
      const auto rs2 = __builtin_amdgcn_permlane16_swap(asu(rs1), asu(rs1), false, false);
      const auto nm2 = __builtin_amdgcn_permlane16_swap(asu(nm1), asu(nm1), false, false);
#pragma unroll
      for (int j = 0; j < 4; ++j) {
        const u32x4 v = *(LAS u32x4*)(lds + SPARE + (lw * 4 + j) * 1024 + l16);
        const float rs = asf(rs2[j & 1]), nm = asf(nm2[j & 1]);
        u32x4 o;
#pragma unroll
        for (int e = 0; e < 4; ++e) o[e] = pk2s(__builtin_fmaf(bflo(v[e]), rs, nm), __builtin_fmaf(bfhi(v[e]), rs, nm));
        st16(rw, WS_XN + ((pm * 4 + lw) * 64 + pn * 4 + j) * 1024 + l16, o);
      }
      if (!late) VBAR(4);
      publish(rw, FL_4 + ((pm * 4 + lw) * 16 + pn) * 4, tag, lane);
      touch(tu < 7 * MB ? w4 + tu : wo + (tu - 6 * MB), tl, lane * 128);
    }
    static_assert(ATT1 == 4, "schedule");
    {
      unsigned v3[7];
#pragma unroll
      for (int i = 0; i < 7; ++i) { const int id = i * 64 + lane; v3[i] = (id / 28) * 7168 + ((id % 28) ^ (((id / 28) >> 1) & 2)) * 16; }
      auto i_w3 = [&](int kt) {
        glds4(ws + WS_XN + (size_t)((pm * 4 + lw) * 64 + kt * 4) * 1024, ldsa + sb + lw * 4096, l16);
        const char* b = w3 + (size_t)(kt * 64 + lw * 16) * 7168 + pn * 448;
#pragma unroll
        for (int i = 0; i < 7; ++i) glds1(b, ldsa + sb + 16384 + lw * 7168 + i * 1024, v3[i]);
        sb = next_slot(sb);
      };
      VBAR(2);
      poll(rw, FL_4 + pm * 256 + (lane & 15) * 16, tag);
      i_w3(0);
#pragma unroll 1
      for (int kt = 1; kt < 16; ++kt) { i_w3(kt); VBAR(11); }
    }
#pragma unroll 1
    for (int st = ATT1; st < 8; ++st) {
      i_att(st);
      if (PF & 8) if (st == 7 && lw == 1) touch(w4 + (size_t)(xcd & 1) * 1792 * 2048 + slow_line(w4), tl, idx * 65536 + lane * 1024);
      if (st == 6 && bid == 0 && lw == 0) {
#pragma unroll 1
        for (int i = 0; i < 4; ++i) poll(rw, FL_1 + (i * 8 + (lane >> 3)) * 2048 + (lane & 7) * 16, tag);
        if (lane == 0) __builtin_amdgcn_raw_buffer_store_b32(tag, rw, FL_E, 0, 16);
      }
      VBAR(8);
    }
    {
      const int kh = pn >> 3;
      const char* a0 = ws + WS_H + (size_t)((pm * 4 + lw) * 224 + kh * 112) * 1024;
      const char* b0 = w4 + (size_t)kh * 1792 * 2048;
      auto i_w4 = [&](int kt) {
        glds4(a0 + kt * 6144, ldsa + sb + lw * 6144, l16);
        glds2(a0 + kt * 6144 + 4096, ldsa + sb + lw * 6144 + 4096, l16);
        const char* b = b0 + (size_t)(kt * 96 + lw * 24) * 2048;
        panel(b + pn * 128, 2048, ldsa + sb + 24576 + lw * 3072, vb2, vb2, 3);
        panel(b + (pn ^ 9) * 128, 2048, ldsa + sb + 36864 + lw * 3072, vb2, vb2, 3);
        sb = next_slot(sb);
      };
      VBAR(0);
      poll(rw, FL_6 + (pm * 16 + kh * 8) * 32 + (lane & 15) * 16, tag);
      i_w4(0);
#pragma unroll 1
      for (int kt = 1; kt < 18; ++kt) { i_w4(kt); VBAR(12); }
      const unsigned fo = FL_5 + pm * 512 + (lane & 31) * 16;
      const u32x4 f = peek(rw, fo);
      glds4(a0 + 18 * 6144, ldsa + sb + lw * 6144, l16);
      const char* b = b0 + (size_t)(18 * 96 + lw * 16) * 2048;
      panel(b + pn * 128, 2048, ldsa + sb + 24576 + lw * 2048, vb2, vb2, 2);
      panel(b + (pn ^ 9) * 128, 2048, ldsa + sb + 36864 + lw * 2048, vb2, vb2, 2);
      sb = next_slot(sb);
      VBAR(8);
      check(f, rw, fo, tag);
    }
#pragma unroll 1
    for (int kt = 0; kt < 8; ++kt) { i_nf(WS_O, wo, kt); VBAR(12); }
    VBAR(0);
    if (CPF) asm volatile(".Lcode_b:");
    return;
  }

  const int wm = wid & 3, wn = wid >> 2;
  const int bm = (wid & 1) * 2, bn = (wid >> 1) & 1;
  auto nohook = [](int) {};
  typedef Gemm<2, 4, 2, 4, DN, PSN, 1, 128, 4, 0, 0> GemmN;
  unsigned tag;
  int sb = 0;
  const char* tp;
  {
    const int j = lane / 20, r = lane - j * 20, c = (idx & 1) * 12 + r - 8, kr = (xcd >> 1) * 16 + (idx >> 1);
    const int k = min(2 + 3 * (wn ? wm : T0 + wm) + j, 15);
    tp = r < 8 ? x + (xcd * 256 + idx * 8 + r) * 2048 + k * 128
               : (c < 8 ? w1 + (k * 64 + kr) * 2048 : wqkv + (k * 64 + kr) * 6144 + ((c >> 3) - 1) * 2048) + (xcd & 1) * 1024 + (c & 7) * 128;
  }
  const unsigned tw = ((xcd >> 1) * 32 + idx) * 8 + (lane >> 3), tc = (xcd & 1) * 1024 + (lane & 7) * 128;
  if (wn == 0) {
    Gemm<2, 4, 2, 4, DN, PSN, 0, 128, 4, 0, 15> g(lds + SLOT, lane, wm, 32768, 0);
    f32x4 acc[2][4];
    u32x2 xt[8] = {};
    {
      const int prow = pm * 128 + pn * 8 + wm * 2 + (lane >> 5);
      u32x4 pa[4], ga[4];
      unsigned ev;
      {
        const unsigned l32 = (lane & 31) * 16, xo = prow * 2048 + l32;
        asm volatile("global_load_dwordx4 %0, %9, %11\n\tglobal_load_dwordx4 %1, %9, %11 offset:512\n\tglobal_load_dwordx4 %2, %9, %11 offset:1024\n\t"
                     "global_load_dwordx4 %3, %9, %11 offset:1536\n\tglobal_load_dwordx4 %4, %10, %12\n\tglobal_load_dwordx4 %5, %10, %12 offset:512\n\t"
                     "global_load_dwordx4 %6, %10, %12 offset:1024\n\tglobal_load_dwordx4 %7, %10, %12 offset:1536"
                     : "=&v"(pa[0]), "=&v"(pa[1]), "=&v"(pa[2]), "=&v"(pa[3]), "=&v"(ga[0]), "=&v"(ga[1]), "=&v"(ga[2]), "=&v"(ga[3]), "=&v"(ev)
                     : "v"(xo), "v"(l32), "s"(x), "s"(gw) : "memory");
        if (wm == 0) asm volatile("buffer_load_dword %0, off, %1, %2 sc1" : "+v"(ev) : "s"(rw), "s"(FL_E) : "memory");
      }
      const int rb0 = 16384 + bn * 4096 + wfrag_off<0, 128>(lane, (wid & 1) * 2), rb1 = rb0 ^ 32;
      const unsigned so = WS_ST1 + (pm * 128 + wm * 32 + (lane & 31)) * 16;
      const unsigned o2 = (lane & 15) == 0 ? 0x3f803f80u : 0u;
      const u32x4 o4 = {o2, o2, o2, o2};
      bf16x8 ones = __builtin_bit_cast(bf16x8, o4);
      asm volatile("" : "+v"(ones));
      f32x4 cs[2] = {};
      u32x2 t[4];
      PRIO(PA);
      u32x4 s0;
      unsigned hd;
      const int xp = (pn % 3) * SLOT + (wm * 32 + (lane & 15)) * 128 + (((lane >> 5) ^ ((lane >> 1) & 7)) << 4) + ((lane >> 4) & 1) * 8;
      LBAR();
      STAMP(1);
      float s = 0.f, s2 = 0.f, mu = 0.f;
#pragma unroll
      for (int kt = 0; kt < 16; ++kt) {
        if (kt == KO) {
          asm volatile("s_waitcnt vmcnt(0)" : "+v"(pa[0]), "+v"(pa[1]), "+v"(pa[2]), "+v"(pa[3]), "+v"(ga[0]), "+v"(ga[1]), "+v"(ga[2]), "+v"(ga[3]), "+v"(ev)::"memory");
          if (wm == 0) *(LAS unsigned*)(lds + TAGW) = ev + 1u;
        }
        if (kt == KO + 1) tag = __builtin_amdgcn_readfirstlane(*(LAS unsigned*)(lds + TAGW));
        if (kt % TS_ == 0 && kt / TS_ < 5 - T0 && kt / TS_ == wm) touch64(tp, ldsa + SPARE + 1024);
        if ((PF & 1) && (kt == PFK || kt == PFK + PFD) && wm == 3) {
          const int kr = (kt == PFK ? 0 : 512) + idx * 16 + (lane & 15), sg = min(lane >> 4, 2);
          touch64((sg == 0 ? w1 + slow_line(w1) + kr * 2048 : wqkv + slow_line(wqkv) + kr * 6144 + (sg - 1) * 2048) + (xcd & 1) * 1024, ldsa + SPARE + 1024);
        }
        if (kt == TS_ * (5 - T0) && wm == 0) touch(wqkv + 4096, ldsa + SPARE + 1024, tw * 6144 + tc);
        if (kt == TS_ * (6 - T0) && wm == 0) touch(w2, ldsa + SPARE + 1024, tw * 2048 + tc);
        SCHED();
        if (kt < KO) {
        } else if (kt == KO) {
#pragma unroll
          for (int i = 0; i < 4; ++i)
#pragma unroll
            for (int e = 0; e < 4; ++e) { s += bflo(pa[i][e]); s2 += bfhi(pa[i][e]); asm volatile("" : "+v"(s), "+v"(s2)); }
        } else if (kt == KO + 1) {
          mu = half_sum(s + s2) * (1.0f / 1024.0f);
          s = s2 = 0.f;
        } else if (kt == KO + 2) {
#pragma unroll
          for (int i = 0; i < 4; ++i)
#pragma unroll
            for (int e = 0; e < 4; ++e) {
              float t0 = bflo(pa[i][e]) - mu, t1 = bfhi(pa[i][e]) - mu;
              asm("" : "+v"(t0)); asm("" : "+v"(t1));
              s += t0 * t0; s2 += t1 * t1;
              asm volatile("" : "+v"(s), "+v"(s2));
            }
        } else if (kt == KO + 3) {
          s = half_sum(s + s2);
          if ((lane & 31) == 0) {
            u32x4 o; o[0] = asu(mu); o[1] = tag; o[2] = asu(__builtin_amdgcn_rsqf(s * (1.0f / 1024.0f) + 1e-5f)); o[3] = tag;
            st16(rw, WS_ST1 + prow * 16, o);
          }
          s = s2 = 0.f;
        } else if (kt < KO + 6) {
#pragma unroll
          for (int i = 2 * (kt - KO) - 8; i < 2 * (kt - KO) - 6; ++i)
#pragma unroll
            for (int e = 0; e < 4; ++e) {
              const unsigned pr = pk2s(bflo(pa[i][e]) * bflo(ga[i][e]), bfhi(pa[i][e]) * bfhi(ga[i][e]));
              s += bflo(pr); s2 += bfhi(pr);
              asm volatile("" : "+v"(s), "+v"(s2));
            }
        } else if (kt == KO + 6) {
          s = half_sum(s + s2);
          if ((lane & 31) == 0) {
            const float gt = rbf(__builtin_amdgcn_rcpf(1.0f + __builtin_amdgcn_exp2f(-1.4426950408889634f * rbf(s))));
            u32x4 o; o[0] = asu(gt); o[1] = tag; o[2] = asu(rbf(1.0f - gt)); o[3] = tag;
            st16(rw, WS_GT + prow * 16, o);
          }
        } else if (kt == TVS) {
          if (PF & 2) if (wm == 1) touch(wqkv + 4096 + slow_line(wqkv), ldsa + SPARE + 1024, (idx * 32 + (lane & 31)) * 6144 + (xcd & 1) * 1024);
        } else if (kt == 11) {
          s0 = ld16(rw, so);
        } else if (kt == 12) {
          const int grp = pn >> 2, sh = (grp & 1) ? -(1 + (grp >> 1)) : (1 + (grp >> 1));
          const int md = pm * 128 + wm * 32 + (lane & 31) + sh, mw = md & 2047;
          mu = (unsigned)md < 2048u ? GS : 0.f;
          hd = WS_HS + ((mw >> 5) * 64 + pn * 4 + ((mw >> 4) & 1)) * 1024 + (mw & 15) * 16;
          asm volatile("" : "+v"(mu), "+v"(hd));
        } else if (kt == 13) {
#pragma unroll 1
          for (int it = 0; it < SPIN_CAP; ++it) {
            if (__all(s0[1] == tag && s0[3] == tag)) break;
            spin_pause();
            s0 = ld16(rw, so);
          }
          u32x4 o; o[0] = s0[0] ^ 0x80000000u; o[1] = asu(asf(s0[2]) * mu); o[2] = o[3] = hd;
          *(LAS u32x4*)(lds + SPARE + 2048 + wm * 512 + (lane & 31) * 16) = o;
        } else if (kt == 14) {
          if (PF & 4) if (wm == 3) touch(w2 + slow_line(w2), ldsa + SPARE + 1024, (idx * 32 + (lane & 31)) * 2048 + (xcd & 1) * 1024);
#pragma unroll
          for (int i = 0; i < 8; ++i) { acc[i >> 2][i & 3] = (f32x4){}; asm volatile("" : "+v"(acc[i >> 2][i & 3])); }
        }
        SCHED();
        if ((kt > 0 && kt < KO) || kt == KO + 1 || (kt > KO + 6 && kt < 15 && kt != 13)) __builtin_amdgcn_s_sleep(CSD);
        if (kt == pn) {
#pragma unroll
          for (int i = 0; i < 8; ++i) xt[i] = *(LAS u32x2*)(lds + (xp ^ ((i >> 2) * 64 + (i & 1) * 32)) + ((i >> 1) & 1) * 2048);
        }
        SCHED();
#pragma unroll
        for (int i = 0; i < 4; ++i) t[i] = rd_tr(lds + (kt % 3) * SLOT + ((i & 2) ? rb1 : rb0) + (i & 1) * 512);
        SCHED();
        if (kt < 15) { LBAR(); SCHED(); }
#pragma unroll
        for (int i = 0; i < 2; ++i) cs[i] = MFMA16(cat(t[2 * i], t[2 * i + 1]), ones, cs[i]);
        SCHED();
      }
      if ((lane & 15) == 0) {
#pragma unroll
        for (int i = 0; i < 2; ++i) *(LAS f32x4*)(lds + SPARE + bn * 256 + (wid & 1) * 128 + i * 64 + lane) = cs[i];
      }
      LBAR();
      PRIO(0);
      SCHED();
    }
    STAMP(2);
    sb = SLOT;
    g.first();
    g.run(acc, sb, 0, 7, [](int kt) { if (kt == 3) WAIT_V(0); });
    STAMP(3);
    g.last(acc, sb);
    STAMP(4);
    const unsigned dv = WS_V + (pn ^ 3) * 262144 + (pm * 4 + wm) * 2048 + (lane >> 4) * 512 + (lane & 8) * 32 + (lane & 7) * 16;
#pragma unroll
    for (int i = 0; i < 4; ++i) st16q(rw, dv + (i >> 1) * 131072 + (i & 1) * 128, pack44(acc[0][i], acc[1][i], 1.0f));
    SCHED();
    STAMP(5);
    bars(1);
#pragma unroll
    for (int s = 0; s < 4; ++s) *(LAS bf16x8*)(lds + SPARE + (wm * 4 + s) * 1024 + lane * 16) = cat(xt[2 * s], xt[2 * s + 1]);
    unsigned fo = FL_2 + (lane < 16 ? (pn ^ 3) * 8192 + 128 + (pm * 4 + wm) * 4 + lane * 512 : bn * 4096 + (wid & 1) * 4 +
                          (lane < 24 ? (pn ^ 2) * 8192 + pm * 8 + (lane - 16) * 512 : (pn ^ 1) * 8192 + (pm >> 1) * 512 + 384 + (pm & 1) * 8)) - lane * 4;
    asm volatile("" : "+v"(fo));
    LBAR();
    bars(PV - 2);
    publish(rw, fo, tag, lane, 25);
    STAMP(6);
    bars(8 - PV);
  } else {
    if (CPF && wm == 1) {
      const char* pc;
      int len;
      asm volatile("s_getpc_b64 %0\n.Lcode_a:\n\ts_mov_b32 %1, .Lcode_b-.Lcode_a" : "=s"(pc), "=s"(len));
      const int o = (lane * 32 + idx) * 128;
      if (o < len) touch(pc, ldsa + SPARE + 1024, o);
    }
    if (wm < T0) touch64(tp, ldsa + SPARE + 1024);
    u32x4 qk[8];
    {
      Gemm<4, 6, 4, 2, DQ, PSQ, 0, 128, 2, 8192, 0> g(lds, lane, bm, 16384, bn * 2);
      f32x4 acc[4][6] = {};
#pragma unroll
      for (int i = 0; i < 24; ++i) asm volatile("" : "+v"(acc[i / 6][i % 6]));
      LBAR();
      STAMP(1);
      g.first();
      g.run(acc, sb, 0, 15, nohook);
      g.last(acc, sb);
      STAMP(2);
      asm volatile("s_waitcnt vmcnt(0)" : "+v"(lane)::"memory");
      tag = __builtin_amdgcn_readfirstlane(*(LAS unsigned*)(lds + TAGW));
      f32x4 cs[2];
      u32x4 s0[4];
#pragma unroll
      for (int mi = 0; mi < 4; ++mi) s0[mi] = *(LAS u32x4*)(lds + SPARE + 2048 + bm * 512 + mi * 256 + (lane & 15) * 16);
#pragma unroll
      for (int ni = 0; ni < 2; ++ni) {
        LAS char* cp = lds + SPARE + (bn * 2 + ni) * 64 + (lane >> 4) * 16;
        const f32x4 c0 = *(LAS f32x4*)cp, c1 = *(LAS f32x4*)(cp + 256);
#pragma unroll
        for (int e = 0; e < 4; ++e) { float t = c0[e] + c1[e]; asm("" : "+v"(t)); cs[ni][e] = t; }
      }
#pragma unroll
      for (int mi = 0; mi < 4; ++mi) {
        const float rs = asf(s0[mi][1]), nmu = asf(s0[mi][0]);
        float hv[8];
#pragma unroll
        for (int e = 0; e < 8; ++e) hv[e] = gelu_s(rs * __builtin_fmaf(nmu, cs[e >> 2][e & 3], acc[mi][e >> 2][e & 3]));
        u32x4 o;
#pragma unroll
        for (int e = 0; e < 4; ++e) o[e] = pk2s(hv[2 * e], hv[2 * e + 1]);
        st16q(rw, s0[mi][2] + bn * 2048 + (lane & 48) * 16, o);
        SCHED();
        if ((E1 >> mi) & 1) bars(1);
        SCHED();
      }
#pragma unroll
      for (int i = 0; i < 4; ++i) {
        qk[i] = pack44(acc[i][4], acc[i][5], 1.0f); qk[4 + i] = pack44(acc[i][2], acc[i][3], 0.17677669529663687f * 1.4426950408889634f);
        asm volatile("" : "+v"(qk[i]), "+v"(qk[4 + i]));
      }
    }
    {
      const unsigned db = WS_Q + ((bn * 64 + pm * 4 + bm) * 2) * 1024 + (lane >> 4) * 512;
      const unsigned dq = db + (pn ^ 1) * 262144 + (lane & 15) * 16, dk = db + 4 * MB + (pn ^ 2) * 262144 + (lane & 8) * 32 + (lane & 7) * 16;
      auto put = [&](int i0, int i1) {
#pragma unroll
        for (int i = i0; i < i1; ++i) st16q(rw, i < 4 ? dk + (i >> 1) * 2048 + (i & 1) * 128 : dq + ((i >> 1) & 1) * 2048 + (i & 1) * 256, qk[i]);
        SCHED();
      };
      constexpr int NB = 8 - __builtin_popcount(E1);
      put(0, QB);
      bars(P1);
      asm volatile("s_waitcnt vmcnt(%0)" ::"n"(QB) : "memory");
      if (lane < 16) __builtin_amdgcn_raw_buffer_store_b32(tag, rw, FL_1 + (pm * 2 + (wid & 1)) * 2048 + lane * 128 + (pn * 2 + bn) * 4, 0, 16);
      STAMP(3);
      const int mrow = pm * 128 + wm * 32 + (lane & 15);
      f32x4 acc[2][4] = {};
      sb = 0;
      GemmN g(lds, lane, wm, 32768, 0);
#pragma unroll
      for (int s = 0; s < 4; ++s) asm volatile("" : "+v"(acc[0][s]), "+v"(acc[1][s]));
      SCHED();
#pragma unroll
      for (int i = 0; i < NB - P1; ++i) {
        put(QB + (8 - QB) * i / (NB - P1), QB + (8 - QB) * (i + 1) / (NB - P1));
        bars(1);
      }
      STAMP(4); STAMP(5);
      g.first();
      g.run(acc, sb, 0, 7, [&](int kt) {
        if (kt == PV - 2) WAIT_V(0);
      });
      u32x4 xv[4];
#pragma unroll
      for (int s = 0; s < 4; ++s) xv[s] = *(LAS u32x4*)(lds + SPARE + (wm * 4 + s) * 1024 + lane * 16);
      SCHED();
      g.last(acc, sb);
      STAMP(6);
      u32x4 o[4];
      const u32x4 o4 = {0x3f803f80u, 0x3f803f80u, 0x3f803f80u, 0x3f803f80u};
      f32x4 sm[2] = {}, sq[2] = {};
#pragma unroll
      for (int s = 0; s < 4; ++s) {
#pragma unroll
        for (int e = 0; e < 4; ++e) {
          const f32x4& c = acc[s & 1][(s >> 1) * 2 + (e >> 1)];
          o[s][e] = pk2s(__builtin_fmaf(c[2 * (e & 1)], GSI, bflo(xv[s][e])), __builtin_fmaf(c[2 * (e & 1) + 1], GSI, bfhi(xv[s][e])));
        }
        const bf16x8 f = __builtin_bit_cast(bf16x8, o[s]);
        sm[s & 1] = MFMA16(__builtin_bit_cast(bf16x8, o4), f, sm[s & 1]);
        sq[s & 1] = MFMA16(f, f, sq[s & 1]);
      }
      if ((lane >> 4) == ((lane >> 2) & 3)) {
#pragma unroll
        for (int mi = 0; mi < 2; ++mi) {
          const float q = (lane & 2) ? ((lane & 1) ? sq[mi][3] : sq[mi][2]) : ((lane & 1) ? sq[mi][1] : sq[mi][0]);
          const float mean = sm[mi][0] * (1.0f / 64.0f);
          u32x4 t; t[0] = asu(mean); t[1] = tag; t[2] = asu(fmaxf(__builtin_fmaf(-mean, sm[mi][0], q), 0.f)); t[3] = tag;
          st16(rw, WS_ST2 + (pn * 2048 + mrow + mi * 16) * 16, t);
        }
      }
#pragma unroll
      for (int s = 0; s < 4; ++s) *(LAS u32x4*)(lds + SPARE + (wm * 4 + s) * 1024 + lane * 16) = o[s];
    }
  }
  STAMP(7);
  Att a;
  a.o = (f32x16){};
  a.l = (f32x4){};
  sb = 2 * SLOT;
  attend<true>(a, lds, sb, ATT1, lane, wid, nohook);
  STAMP(8);
  if (wn == 0) w3_phase<4>(lds, sb, rw, lane, bm, bn, pm, pn, ws);
  else w3_phase<2>(lds, sb, rw, lane, bm, bn, pm, pn, ws);
  STAMP(10);
  asm volatile("" : "+v"(lane), "+v"(a.m));
  a.l[1] = a.l[2] = a.l[3] = 0.f;
  const int mb = qg * 8 + wid;
  const unsigned go = WS_GT + (mb * 32 + (lane & 31)) * 16;
  u32x2 gv = ld8(rw, go);
  attend<false>(a, lds, sb, 8 - ATT1, lane, wid, [&](int st) {
    if (st == P6) publish(rw, FL_6 + ((pm * 16 + pn) * 8 + wid) * 4, tag, lane);
  });
  STAMP(11);
  if (__builtin_expect(__any(!(a.l[0] <= 1.152921504606847e18f) || gv[1] != tag), 0)) {
    if (__any(!(a.l[0] <= 1.152921504606847e18f))) attend_exact(a, rw, head, lane);
#pragma unroll 1
    for (int it = 0; it < SPIN_CAP; ++it) {
      if (__all(gv[1] == tag)) break;
      spin_pause();
      gv = ld8(rw, go);
    }
  }
  auto put_o = [&]() {
    const float c = asf(gv[0]) * __builtin_amdgcn_rcpf(asf(__builtin_amdgcn_permlane32_swap(asu(a.l[0]), asu(a.l[0]), false, false)[0]));
    const unsigned d = WS_O + (mb * 64 + head * 2 + ((lane >> 4) & 1)) * 1024 + ((lane >> 5) * 16 + (lane & 15)) * 16;
#pragma unroll
    for (int s = 0; s < 2; ++s) st16(rw, d + s * 512, pack8(a.o, s, c));
  };
  if (wn == 0) {
    PRIO(0);
    put_o();
    STAMP(12);
    bars(4);
    publish(rw, FL_5 + ((qg * 8 + wid) * 32 + head) * 4 + lane * 508, tag, lane, 2);
    bars(1);
    f32x4 acc[2][4] = {};
    sb = 0;
    GemmN g(lds, lane, wm, 32768, 0);
    char* op = out + (size_t)(pm * 128 + wm * 32 + (lane >> 3)) * 2048 + pn * 128 + (lane & 7) * 16;
    asm volatile("" : "+v"(op));
#pragma unroll
    for (int s = 0; s < 4; ++s) asm volatile("" : "+v"(acc[0][s]), "+v"(acc[1][s]));
    unsigned tv = 0;
    if (PF & 8) if (wid == 0) tv = touch_v(w4 + (size_t)((xcd & 1) * 1792 + 1024) * 2048 + slow_line(w4), idx * 49152 + min(lane, 47) * 1024);
    bars(7);
    if (PF & 16) if (wid == 1) tv = touch_v(wo + slow_line(wo), (idx * 32 + (lane & 31)) * 2048 + (xcd & 1) * 1024);
    bars(6);
    asm volatile("s_waitcnt vmcnt(0)" : "+v"(tv)::"memory");
    PRIO(PG);
    bars(1);
    STAMP(13);
    g.first();
    g.run(acc, sb, 0, 7, nohook);
    g.last(acc, sb);
    STAMP(14);
#pragma unroll
    for (int s = 0; s < 4; ++s) {
      const int j = s >> 1, mi = s & 1;
      const u32x4 u = *(LAS u32x4*)(lds + SPARE + (wm * 4 + j * 2 + mi) * 1024 + lane * 16);
      const f32x4 pa = f32x4{bflo(u[0]), bfhi(u[0]), bflo(u[1]), bfhi(u[1])} + acc[mi][2 * j], pb = f32x4{bflo(u[2]), bfhi(u[2]), bflo(u[3]), bfhi(u[3])} + acc[mi][2 * j + 1];
      const u32x4 o = pack44(pa, pb, 1.0f);
      LAS char* tp = lds + wm * 4608 + (mi * 16 + (lane & 15)) * 144 + j * 64 + (lane >> 4) * 8;
      *(LAS u32x2*)tp = u32x2{o[0], o[1]};
      *(LAS u32x2*)(tp + 32) = u32x2{o[2], o[3]};
    }
#pragma unroll
    for (int s = 0; s < 4; ++s) {
      const u32x4 o = *(LAS u32x4*)(lds + wm * 4608 + (s * 8 + (lane >> 3)) * 144 + (lane & 7) * 16);
      asm volatile("global_store_dwordx4 %0, %1, off" OST ::"v"(op + s * 16384), "v"(o) : "memory");
    }
  } else {
    f32x4 acc[4][4] = {};
    u32x2 o1;
    const unsigned oo = WS_GT + (pm * 128 + bm * 32) * 16 + 8;
    {
      Gemm<4, 4, 4, 3, D4, PS4, 1, 128, 2, 12288, 0> g(lds + sb, lane, bm, 24576, bn * 2);
      PRIO(PG);
      g.first();
      put_o();
      { int l = lane; asm volatile("" : "+v"(l)); o1 = ld8(rw, oo + l * 16); }
      SCHED();
      STAMP(12);
      g.run(acc, sb, 0, 18, [&](int kt) {
        if (kt == 2) WAIT_V(0);
      });
      g.last<2>(acc, sb);
      PRIO(0);
    }
    STAMP(13);
#pragma unroll 1
    for (int it = 0; it < SPIN_CAP; ++it) {
      if (__all(o1[1] == tag)) break;
      spin_pause();
      o1 = ld8(rw, oo + lane * 16);
    }
    asm volatile("" : "+v"(o1));
#pragma unroll
    for (int i = 0; i < 4; ++i) st16(rw, WS_P + ((pm * 16 + pn) * 16 + wm * 4 + i) * 1024 + lane * 16, pack44(acc[i][2], acc[i][3], 1.0f));
    SCHED();
    bars(1);
#pragma unroll
    for (int mi = 0; mi < 4; ++mi) {
      const u32x4 xv = *(LAS u32x4*)(lds + SPARE + ((bm + (mi >> 1)) * 4 + bn * 2 + (mi & 1)) * 1024 + lane * 16);
#pragma unroll
      for (int e = 0; e < 8; ++e) {
        float t = __builtin_fmaf(acc[mi][e >> 2][e & 3], GSI, (e & 1) ? bfhi(xv[e >> 1]) : bflo(xv[e >> 1]));
        asm volatile("" : "+v"(t));
        acc[mi][e >> 2][e & 3] = t;
      }
    }
    SCHED();
    bars(1);
    unsigned ov[4];
#pragma unroll
    for (int mi = 0; mi < 4; ++mi) ov[mi] = __builtin_amdgcn_ds_bpermute(mi * 64 + (lane & 15) * 4, o1[0]);
#pragma unroll
    for (int i = 0; i < 32; ++i) {
      float t = acc[i >> 3][(i >> 2) & 1][i & 3] * asf(ov[i >> 3]);
      asm volatile("" : "+v"(t));
      acc[i >> 3][(i >> 2) & 1][i & 3] = t;
    }
    SCHED();
    bars(PW);
    publish(rw, FL_7 + ((pm * 16 + pn) * 4 + wm) * 16, tag, lane, 4);
    bars(PX);
    const unsigned fo = FL_7 + ((pm * 16 + (pn ^ 9)) * 4 + wm) * 16;
    int nb = 5 - PW - PX;
#pragma unroll 1
    for (int it = 0; it < SPIN_CAP; ++it) {
      const u32x4 f = peek(rw, fo);
      if (nb > PY) { bars(1); --nb; } else spin_pause();
      WAIT_V(0);
      u32x4 fv = f;
      asm volatile("" : "+v"(fv)::"memory");
      if (__all(fv[0] == tag)) break;
    }
    u32x4 pv[4];
#pragma unroll
    for (int i = 0; i < 4; ++i) pv[i] = ld16(rw, WS_P + ((pm * 16 + (pn ^ 9)) * 16 + wm * 4 + i) * 1024 + lane * 16);
    SCHED();
    { const int k = min(nb, PZ); bars(k); nb -= k; }
    SCHED();
    STAMP(14);
#pragma unroll
    for (int mi = 0; mi < 4; ++mi) {
      const float og = asf(ov[mi]) * GSI;
      u32x4 u;
#pragma unroll
      for (int e = 0; e < 4; ++e)
        u[e] = pk2s(__builtin_fmaf(bflo(pv[mi][e]), og, acc[mi][e >> 1][2 * (e & 1)]), __builtin_fmaf(bfhi(pv[mi][e]), og, acc[mi][e >> 1][2 * (e & 1) + 1]));
      *(LAS u32x4*)(lds + SPARE + ((bm + (mi >> 1)) * 4 + bn * 2 + (mi & 1)) * 1024 + lane * 16) = u;
      if (US && mi == 1) { SCHED(); if (nb > 0) { bars(1); --nb; } SCHED(); }
    }
    SCHED();
    bars(nb);
    LBAR();
  }
  STAMP(15);
}

extern "C" void kernel_launch(void* const* d_in, const int* in_sizes, int n_in, void* d_out, int out_size, void* d_ws,
                              size_t ws_size, hipStream_t stream) {
  (void)hipFuncSetAttribute((const void*)k_mega, hipFuncAttributeMaxDynamicSharedMemorySize, LDS_BYTES);
  k_mega<<<256, 768, LDS_BYTES, stream>>>((const char*)d_in[0], (const char*)d_in[1], (const char*)d_in[2], (const char*)d_in[4],
                                          (const char*)d_in[5], (const char*)d_in[6], (const char*)d_in[7], (const char*)d_in[8],
                                          (char*)d_ws, (char*)d_out);
}
```
